```python
import torch

def _softmax(x, axis=-1):
    return torch.softmax(x, dim=axis)
def _gelu(x):
    return torch.nn.functional.gelu(x, approximate="tanh")
def _silu(x):
    return torch.nn.functional.silu(x)
def _sigmoid(x):
    return torch.sigmoid(x)
def _layernorm(x, axis=-1, eps=1e-5):
    m = x.mean(dim=axis, keepdim=True)
    v = x.var(dim=axis, keepdim=True, unbiased=False)
    return (x - m) / torch.sqrt(v + eps)
def _scatter_add(tgt, idx, src, axis=0):
    return tgt.clone().index_add_(axis, idx, src)
def _segment_sum(src, seg, n):
    out = torch.zeros((n,) + src.shape[1:], dtype=src.dtype)
    return out.index_add_(0, seg, src)
def _pad(x, pw):
    flat = tuple(v for pair in reversed(pw) for v in pair)
    return torch.nn.functional.pad(x, flat)

kernel_name = 'gated_mix__gqa_block_cached__compressed_kv_cla__decode'

def setup_inputs():

    _w = (lambda a: (lambda t: t.to(torch.bfloat16) if t.is_floating_point() else t)(torch.as_tensor(a)))
    def _A_setup():
        B, Sq, D = 2, 1, 576
        Hq, Hkv, Sk, Dh = 32, 16, 4096, 128
        assert Hq % Hkv == 0, (Hq, Hkv)
        return {
            "x": _w((lambda *s: torch.randn(*s, dtype=torch.bfloat16) * 0.05)(B, Sq, D)),
            "k_cache": _w((lambda *s: torch.randn(*s, dtype=torch.bfloat16) * 6.4)(B, Hkv, Sk, Dh)),
            "v_cache": _w((lambda *s: torch.randn(*s, dtype=torch.bfloat16) * 0.8)(B, Hkv, Sk, Dh)),
            "wq": _w((lambda *s: torch.randn(*s, dtype=torch.bfloat16) * 6.4)(D, Hq * Dh) * (D ** -0.5)),
            "wo": _w((lambda *s: torch.randn(*s, dtype=torch.bfloat16) * 0.8)(Hq * Dh, D) * ((Hq * Dh) ** -0.5)),
        }
    _A_in = _A_setup()

    def _B_setup():
        B, H, Sq, Sk, D, M = 2, 32, 1, 4096, 128, 576
        Dc = 4 * D
        return {
            "x": _w((lambda *s: torch.randn(*s, dtype=torch.bfloat16) * 0.05)(B, Sq, M)),
            "c_kv": _w((lambda *s: torch.randn(*s, dtype=torch.bfloat16) * 0.8)(B, Sk, Dc)),
            "w_q": _w((lambda *s: torch.randn(*s, dtype=torch.bfloat16) * 6.4)(M, H, D) * (M ** -0.5)),
            "w_uk": _w((lambda *s: torch.randn(*s, dtype=torch.bfloat16) * 6.4)(Dc, H, D) * (Dc ** -0.5)),
            "w_uv": _w((lambda *s: torch.randn(*s, dtype=torch.bfloat16) * 0.8)(Dc, H, D) * (Dc ** -0.5)),
            "w_o": _w((lambda *s: torch.randn(*s, dtype=torch.bfloat16) * 0.8)(H, D, M) * ((H * D) ** -0.5)),
        }
    _B_in = _B_setup()

    return {
        "A_x": _A_in["x"],
        "A_k_cache": _A_in["k_cache"],
        "A_v_cache": _A_in["v_cache"],
        "A_wq": _A_in["wq"],
        "A_wo": _A_in["wo"],
        "B_x": _B_in["x"],
        "B_c_kv": _B_in["c_kv"],
        "B_w_q": _B_in["w_q"],
        "B_w_uk": _B_in["w_uk"],
        "B_w_uv": _B_in["w_uv"],
        "B_w_o": _B_in["w_o"],
        "gate_w": _w((lambda *s: torch.randn(*s, dtype=torch.bfloat16) * 0.05)(576) * 16.0),
    }

def reference(A_x, A_k_cache, A_v_cache, A_wq, A_wo, B_x, B_c_kv, B_w_q, B_w_uk, B_w_uv, B_w_o, gate_w):
    def _A_ref(x, k_cache, v_cache, wq, wo):
        B, Sq, D = x.shape
        Hq, Dh = 32, 128
        Hkv = k_cache.shape[1]
        rep = Hq // Hkv
        q = (lambda x, *d: x.permute(*d))((x @ wq).reshape(B, Sq, Hq, Dh), 0, 2, 1, 3)
        k = (lambda x, n, axis: torch.repeat_interleave(x, n, dim=axis))(k_cache, rep, axis=1)
        v = (lambda x, n, axis: torch.repeat_interleave(x, n, dim=axis))(v_cache, rep, axis=1)
        scores = torch.einsum("bhqd,bhkd->bhqk", q, k) * (Dh ** -0.5)
        p = _softmax(scores, axis=-1)
        o = torch.einsum("bhqk,bhkd->bhqd", p, v)
        return (lambda x, *d: x.permute(*d))(o, 0, 2, 1, 3).reshape(B, Sq, Hq * Dh) @ wo

    def _B_ref(x, c_kv, w_q, w_uk, w_uv, w_o):
        B, Sq, M = x.shape[0], x.shape[1], x.shape[2]
        Sk = c_kv.shape[1]
        H, D = w_q.shape[1], w_q.shape[2]
        q = torch.einsum("bqm,mhd->bhqd", x, w_q)
        k = torch.einsum("bkc,chd->bhkd", c_kv, w_uk)
        v = torch.einsum("bkc,chd->bhkd", c_kv, w_uv)
        scores = torch.einsum("bhqd,bhkd->bhqk", q, k) * (D ** -0.5)
        off = Sk - Sq
        qi = torch.arange(Sq)[:, None]
        kj = torch.arange(Sk)[None, :]
        cmask = kj <= (qi + off)
        scores = torch.where(cmask[None, None], scores, -1e9)
        p = _softmax(scores, axis=-1)
        ctx = torch.einsum("bhqk,bhkd->bhqd", p, v)
        return torch.einsum("bhqd,hdm->bqm", ctx, w_o)

    x = A_x
    g = _sigmoid((x * gate_w).sum(-1, keepdims=True))
    a = _A_ref(x, A_k_cache, A_v_cache, A_wq, A_wo)
    b = _B_ref(x, B_c_kv, B_w_q, B_w_uk, B_w_uv, B_w_o)
    return g * a + (1 - g) * b
```

```cpp
#include <hip/hip_runtime.h>
#include <hip/hip_bf16.h>
#include <stdint.h>

typedef unsigned short u16;
typedef unsigned u32x4 __attribute__((ext_vector_type(4)));
__device__ __forceinline__ float bf2f(u16 v) { return __uint_as_float(((unsigned)v) << 16); }
__device__ __forceinline__ float lo2f(unsigned v) { return __uint_as_float(v << 16); }
__device__ __forceinline__ float hi2f(unsigned v) { return __uint_as_float(v & 0xffff0000u); }
__device__ __forceinline__ float rbf(float f) { return __bfloat162float(__float2bfloat16(f)); }

constexpr int DM = 576, HD = 4096, SK = 4096;
constexpr float SCALE = 0.08838834764831845f;

__global__ void k_qproj(const u16* x, const u16* wq, const u16* w_q, float* q) {
    __shared__ float red[4][2][64];
    const u16* W = blockIdx.y ? w_q : wq;
    int col = blockIdx.x * 64 + (threadIdx.x & 63);
    int wv = threadIdx.x >> 6;
    float a0 = 0, a1 = 0;
    for (int m = wv; m < DM; m += 4) {
        float w = bf2f(W[(size_t)m * HD + col]);
        a0 += bf2f(x[m]) * w; a1 += bf2f(x[DM + m]) * w;
    }
    red[wv][0][threadIdx.x & 63] = a0; red[wv][1][threadIdx.x & 63] = a1;
    __syncthreads();
    if (threadIdx.x < 128) {
        int b = threadIdx.x >> 6, l = threadIdx.x & 63;
        float s = red[0][b][l] + red[1][b][l] + red[2][b][l] + red[3][b][l];
        q[((size_t)blockIdx.y * 2 + b) * HD + blockIdx.x * 64 + l] = rbf(s);
    }
}
__global__ void k_qabs(const float* qB, const u16* w_uk, float* qabs) {
    int c = blockIdx.x, t = threadIdx.x, h = t >> 3, dc = t & 7;
    const u16* w = w_uk + (size_t)c * HD + h * 128 + dc * 16;
    float a0 = 0, a1 = 0;
    for (int j = 0; j < 16; ++j) { float wv = bf2f(w[j]); a0 += wv * qB[h * 128 + dc * 16 + j]; a1 += wv * qB[HD + h * 128 + dc * 16 + j]; }
    for (int o = 1; o < 8; o <<= 1) { a0 += __shfl_xor(a0, o); a1 += __shfl_xor(a1, o); }
    if (dc == 0) { qabs[(size_t)(0 * 32 + h) * 512 + c] = a0; qabs[(size_t)(1 * 32 + h) * 512 + c] = a1; }
}
__global__ void k_scoresA(const float* qA, const u16* kc, float* sc) {
    int bk = blockIdx.x, b = bk >> 4, kvh = bk & 15, k = blockIdx.y * 256 + threadIdx.x;
    const u32x4* kp = (const u32x4*)(kc + ((size_t)bk * SK + k) * 128);
    const float* q0 = qA + (size_t)b * HD + (kvh * 2) * 128; const float* q1 = q0 + 128;
    float s0 = 0, s1 = 0;
    for (int i = 0; i < 16; ++i) { u32x4 v = kp[i];
        for (int j = 0; j < 4; ++j) { float a = lo2f(v[j]), c = hi2f(v[j]); int d = i * 8 + j * 2;
            s0 += a * q0[d] + c * q0[d + 1]; s1 += a * q1[d] + c * q1[d + 1]; } }
    sc[((size_t)b * 32 + kvh * 2) * SK + k] = s0 * SCALE; sc[((size_t)b * 32 + kvh * 2 + 1) * SK + k] = s1 * SCALE;
}
__global__ void k_scoresB(const float* qabs, const u16* ckv, float* sc) {
    int b = blockIdx.y, k = blockIdx.x * 8 + (threadIdx.x & 7), h = threadIdx.x >> 3;
    const u32x4* kp = (const u32x4*)(ckv + ((size_t)b * SK + k) * 512);
    const float* q = qabs + ((size_t)b * 32 + h) * 512;
    float s = 0;
    for (int i = 0; i < 64; ++i) { u32x4 v = kp[i];
        for (int j = 0; j < 4; ++j) { int d = i * 8 + j * 2; s += lo2f(v[j]) * q[d] + hi2f(v[j]) * q[d + 1]; } }
    sc[((size_t)b * 32 + h) * SK + k] = s * SCALE;
}
__global__ void k_softmax(float* sc) {
    __shared__ float red[4];
    float* r = sc + (size_t)blockIdx.x * SK;
    float m = -INFINITY;
    for (int k = threadIdx.x; k < SK; k += 256) m = fmaxf(m, r[k]);
    for (int o = 32; o; o >>= 1) m = fmaxf(m, __shfl_xor(m, o));
    if ((threadIdx.x & 63) == 0) red[threadIdx.x >> 6] = m;
    __syncthreads();
    m = fmaxf(fmaxf(red[0], red[1]), fmaxf(red[2], red[3]));
    __syncthreads();
    float s = 0;
    for (int k = threadIdx.x; k < SK; k += 256) { float e = __expf(r[k] - m); r[k] = e; s += e; }
    for (int o = 32; o; o >>= 1) s += __shfl_xor(s, o);
    if ((threadIdx.x & 63) == 0) red[threadIdx.x >> 6] = s;
    __syncthreads();
    s = red[0] + red[1] + red[2] + red[3];
    float inv = 1.f / s;
    for (int k = threadIdx.x; k < SK; k += 256) r[k] *= inv;
}
__global__ void k_pvA(const float* p, const u16* vc, float* o) {
    __shared__ float red[8][128];
    int bh = blockIdx.x, b = bh >> 5, h = bh & 31, d = threadIdx.x & 127, kg = threadIdx.x >> 7;
    const u16* v = vc + ((size_t)(b * 16 + (h >> 1)) * SK) * 128 + d;
    const float* pr = p + (size_t)bh * SK;
    float a = 0;
    for (int k = kg; k < SK; k += 8) a += pr[k] * bf2f(v[(size_t)k * 128]);
    red[kg][d] = a;
    __syncthreads();
    if (threadIdx.x < 128) { float s = 0; for (int i = 0; i < 8; ++i) s += red[i][d]; o[(size_t)bh * 128 + d] = rbf(s); }
}
__global__ void k_pvB(const float* p, const u16* ckv, float* cc) {
    __shared__ float red[2][512];
    int bh = blockIdx.x, b = bh >> 5, c = threadIdx.x & 511, kg = threadIdx.x >> 9;
    const u16* v = ckv + ((size_t)b * SK) * 512 + c;
    const float* pr = p + (size_t)bh * SK;
    float a = 0;
    for (int k = kg; k < SK; k += 2) a += pr[k] * bf2f(v[(size_t)k * 512]);
    red[kg][c] = a;
    __syncthreads();
    if (threadIdx.x < 512) cc[(size_t)bh * 512 + c] = red[0][c] + red[1][c];
}
__global__ void k_uv(const float* cc, const u16* w_uv, float* o) {
    __shared__ float red[4][128];
    int bh = blockIdx.x, h = bh & 31, d = threadIdx.x & 127, cg = threadIdx.x >> 7;
    float a = 0;
    for (int c = cg; c < 512; c += 4) a += cc[(size_t)bh * 512 + c] * bf2f(w_uv[(size_t)c * HD + h * 128 + d]);
    red[cg][d] = a;
    __syncthreads();
    if (threadIdx.x < 128) o[(size_t)bh * 128 + d] = rbf(red[0][d] + red[1][d] + red[2][d] + red[3][d]);
}
__global__ void k_out(const float* oA, const float* oB, const u16* wo, const u16* w_o, const u16* x, const u16* gw, __hip_bfloat16* out) {
    __shared__ float red[2][16][64];
    __shared__ float gsh;
    int b = blockIdx.y, col = blockIdx.x * 64 + (threadIdx.x & 63), rg = threadIdx.x >> 6;
    float a = 0, c = 0;
    for (int r = rg; r < HD; r += 16) { a += oA[(size_t)b * HD + r] * bf2f(wo[(size_t)r * DM + col]); c += oB[(size_t)b * HD + r] * bf2f(w_o[(size_t)r * DM + col]); }
    red[0][rg][threadIdx.x & 63] = a; red[1][rg][threadIdx.x & 63] = c;
    if (threadIdx.x < 64) {
        float s = 0;
        for (int m = threadIdx.x; m < DM; m += 64) s += rbf(bf2f(x[b * DM + m]) * bf2f(gw[m]));
        for (int o = 32; o; o >>= 1) s += __shfl_xor(s, o);
        if (threadIdx.x == 0) gsh = rbf(1.f / (1.f + __expf(-rbf(s))));
    }
    __syncthreads();
    if (threadIdx.x < 64) {
        float sa = 0, sb = 0;
        for (int i = 0; i < 16; ++i) { sa += red[0][i][threadIdx.x]; sb += red[1][i][threadIdx.x]; }
        float g = gsh;
        float r = rbf(rbf(g * rbf(sa)) + rbf(rbf(1.f - g) * rbf(sb)));
        out[b * DM + col] = __float2bfloat16(r);
    }
}

extern "C" void kernel_launch(void* const* d_in, const int* in_sizes, int n_in, void* d_out, int out_size, void* d_ws, size_t ws_size, hipStream_t stream) {
    const u16* x = (const u16*)d_in[0]; const u16* kc = (const u16*)d_in[1]; const u16* vc = (const u16*)d_in[2];
    const u16* wq = (const u16*)d_in[3]; const u16* wo = (const u16*)d_in[4];
    const u16* ckv = (const u16*)d_in[6]; const u16* w_q = (const u16*)d_in[7]; const u16* w_uk = (const u16*)d_in[8];
    const u16* w_uv = (const u16*)d_in[9]; const u16* w_o = (const u16*)d_in[10]; const u16* gw = (const u16*)d_in[11];
    float* ws = (float*)d_ws;
    float* q = ws;
    float* qabs = q + 4 * HD;
    float* sc = qabs + 2 * 32 * 512;
    float* oA = sc + 128 * SK;
    float* cc = oA + 2 * HD;
    float* oB = cc + 64 * 512;
    k_qproj<<<dim3(64, 2), 256, 0, stream>>>(x, wq, w_q, q);
    k_qabs<<<512, 256, 0, stream>>>(q + 2 * HD, w_uk, qabs);
    k_scoresA<<<dim3(32, 16), 256, 0, stream>>>(q, kc, sc);
    k_scoresB<<<dim3(512, 2), 256, 0, stream>>>(qabs, ckv, sc + 64 * SK);
    k_softmax<<<128, 256, 0, stream>>>(sc);
    k_pvA<<<64, 1024, 0, stream>>>(sc, vc, oA);
    k_pvB<<<64, 1024, 0, stream>>>(sc + 64 * SK, ckv, cc);
    k_uv<<<64, 512, 0, stream>>>(cc, w_uv, oB);
    k_out<<<dim3(9, 2), 1024, 0, stream>>>(oA, oB, wo, w_o, x, gw, (__hip_bfloat16*)d_out);
}
```

```cpp
#include <hip/hip_runtime.h>
#include <hip/hip_bf16.h>
#include <stdint.h>

typedef unsigned short u16;
typedef unsigned u32x4 __attribute__((ext_vector_type(4)));
typedef unsigned u32x2 __attribute__((ext_vector_type(2)));
typedef float f32x4 __attribute__((ext_vector_type(4)));
typedef float f32x16 __attribute__((ext_vector_type(16)));
typedef short s16x4 __attribute__((ext_vector_type(4)));
typedef short bf16x8 __attribute__((ext_vector_type(8)));
typedef __bf16 bf16x2 __attribute__((ext_vector_type(2)));
#define GAS __attribute__((address_space(1)))
#define LAS __attribute__((address_space(3)))
#define DI __device__ __forceinline__
#define RLX_AGENT __ATOMIC_RELAXED, __HIP_MEMORY_SCOPE_AGENT

#define WIN 4
#define N_PA 4
#define N_PB 2
#define N_BA 4
#define N_PURE 6
static_assert(N_PA + N_PB + N_BA + N_PURE == 16, "chunks per wave must add up: 2 x 8 waves x 16 = 256 chunks of 16 keys");

constexpr int DM = 576, HD = 4096, SK = 4096;
constexpr float C2 = 0.08838834764831845f * 1.4426950408889634f;

constexpr int EP_OFF = 0;
constexpr int QABS_FLAG = 1024;
constexpr int BP_FLAG = 2048;
constexpr int QA_OFF = 4096;
constexpr int QBX_OFF = QA_OFF + 32768;
constexpr int QABS_OFF = QBX_OFF + 32768;
constexpr int CTX_OFF = QABS_OFF + 65536;
constexpr int AST_OFF = CTX_OFF + 65536;
constexpr int BST_OFF = AST_OFF + 8192;
constexpr int AP_OFF = BST_OFF + 16384;
constexpr int OUTP_OFF = AP_OFF + 524288;
constexpr int BP_OFF = OUTP_OFF + 72 * 16384;
constexpr int WS_END = BP_OFF + 4194304;
constexpr int STAMP_OFF = 8 << 20;

constexpr int L_R0 = 0;
constexpr int L_WO = 65536;
constexpr int L_QB = 131072;
constexpr int L_M = 139264;
constexpr int M_MRG = L_M;
constexpr int M_RED = L_M + 8704;
constexpr int M_QB = L_M + 12800;
constexpr int M_QA = L_M + 13312;
constexpr int M_O = L_M + 14336;
constexpr int M_FIN = L_M + 14848;
constexpr int LDS_BYTES = 155648;

struct Params {
    const u16 *x, *kc, *vc, *wq, *wo, *ckv, *w_q, *w_uk, *w_uv, *w_o, *gw;
    u16* out;
    unsigned char* ws;
};

DI float bf2f(u16 v) { return __uint_as_float(((unsigned)v) << 16); }
DI float lo2f(unsigned v) { return __uint_as_float(v << 16); }
DI float hi2f(unsigned v) { return __uint_as_float(v & 0xffff0000u); }
DI unsigned pk2(float lo, float hi) { bf16x2 p; p[0] = (__bf16)lo; p[1] = (__bf16)hi; return __builtin_bit_cast(unsigned, p); }
DI float rbf(float f) { return (float)(__bf16)f; }
DI u32x4 ldnt(const u16* p) { return __builtin_nontemporal_load((const GAS u32x4*)p); }
DI float dot2(unsigned k, unsigned q, float acc) { return __builtin_amdgcn_fdot2_f32_bf16(__builtin_bit_cast(bf16x2, k), __builtin_bit_cast(bf16x2, q), acc, false); }
template <int CTRL> DI float dpp(float x) { return __builtin_bit_cast(float, __builtin_amdgcn_mov_dpp(__builtin_bit_cast(int, x), CTRL, 0xf, 0xf, true)); }
constexpr int XOR1 = 0xB1, XOR2 = 0x4E, XOR7 = 0x141, XOR8 = 0x128, ROR4 = 0x124, BC = 0x150;
DI float x16_sum(float x) { auto s = __builtin_amdgcn_permlane16_swap(__float_as_uint(x), __float_as_uint(x), false, false); return __uint_as_float(s[0]) + __uint_as_float(s[1]); }
DI float x32_sum(float x) { auto s = __builtin_amdgcn_permlane32_swap(__float_as_uint(x), __float_as_uint(x), false, false); return __uint_as_float(s[0]) + __uint_as_float(s[1]); }
DI float x16_max(float x) { auto s = __builtin_amdgcn_permlane16_swap(__float_as_uint(x), __float_as_uint(x), false, false); return fmaxf(__uint_as_float(s[0]), __uint_as_float(s[1])); }
DI float x32_max(float x) { auto s = __builtin_amdgcn_permlane32_swap(__float_as_uint(x), __float_as_uint(x), false, false); return fmaxf(__uint_as_float(s[0]), __uint_as_float(s[1])); }
#define VMCNT(n) asm volatile("s_waitcnt vmcnt(" #n ")" ::: "memory")
#define VMCNTX(n) VMCNT(n)
DI void bar() { asm volatile("s_waitcnt lgkmcnt(0)" ::: "memory"); __builtin_amdgcn_s_barrier(); asm volatile("" ::: "memory"); }
DI void glds16(const void* gsrc, unsigned lds_dst) {
    unsigned keep;
    asm volatile("s_mov_b32 %0, m0\n\ts_mov_b32 m0, %2\n\ts_nop 0\n\tglobal_load_lds_dwordx4 %1, off nt\n\ts_mov_b32 m0, %0" : "=&s"(keep) : "v"(gsrc), "s"(lds_dst) : "memory");
}
typedef __amdgpu_buffer_rsrc_t rsrc_t;
DI u32x4 ld_sc1(rsrc_t rs, int off) { return __builtin_amdgcn_raw_buffer_load_b128(rs, off, 0, 16); }
DI u32x2 ld8_sc1(rsrc_t rs, int off) { return __builtin_amdgcn_raw_buffer_load_b64(rs, off, 0, 16); }
DI unsigned ld4_sc1(rsrc_t rs, int off) { return __builtin_amdgcn_raw_buffer_load_b32(rs, off, 0, 16); }
DI void st_sc1(rsrc_t rs, int off, u32x4 v) { __builtin_amdgcn_raw_buffer_store_b128(v, rs, off, 0, 16); }
DI void st8_sc1(rsrc_t rs, int off, u32x2 v) { __builtin_amdgcn_raw_buffer_store_b64(v, rs, off, 0, 16); }
DI void st4_sc1(rsrc_t rs, int off, unsigned v) { __builtin_amdgcn_raw_buffer_store_b32(v, rs, off, 0, 16); }
DI bool spin_over(unsigned long long t0) { __builtin_amdgcn_s_sleep(1); return (__builtin_amdgcn_s_memrealtime() - t0) > 5000000ull; }
DI unsigned fswz(unsigned row) { return ((row & 3u) << 2) | ((row >> 2) & 3u); }

#define STAMP(i) do {} while (0)

struct QProj { u32x4 wv[9]; float xs[2][9]; };
DI void qproj_issue(QProj& s, const u16* x, const u16* W, int w, int lane) {
    const int gq = lane >> 3, ch = lane & 7;
#pragma unroll
    for (int i = 0; i < 9; ++i) { const int row = (w * 9 + i) * 8 + gq; s.xs[0][i] = bf2f(x[row]); s.xs[1][i] = bf2f(x[DM + row]); }
#pragma unroll
    for (int i = 0; i < 9; ++i) { const int row = (w * 9 + i) * 8 + gq; s.wv[i] = ldnt(W + (size_t)row * HD + ch * 8); }
}
DI u32x2 qproj_finish(QProj& s, LAS unsigned char* L, int w, int lane, int tid) {
    float acc[2][8];
#pragma unroll
    for (int k = 0; k < 8; ++k) { acc[0][k] = 0.f; acc[1][k] = 0.f; }
#pragma unroll
    for (int i = 0; i < 9; ++i)
#pragma unroll
        for (int k = 0; k < 4; ++k) {
            const float lo = lo2f(s.wv[i][k]), hi = hi2f(s.wv[i][k]);
            acc[0][2 * k] = fmaf(s.xs[0][i], lo, acc[0][2 * k]); acc[0][2 * k + 1] = fmaf(s.xs[0][i], hi, acc[0][2 * k + 1]);
            acc[1][2 * k] = fmaf(s.xs[1][i], lo, acc[1][2 * k]); acc[1][2 * k + 1] = fmaf(s.xs[1][i], hi, acc[1][2 * k + 1]);
        }
#pragma unroll
    for (int b = 0; b < 2; ++b)
#pragma unroll
        for (int k = 0; k < 8; ++k) { float v = acc[b][k]; v += dpp<XOR8>(v); v = x16_sum(v); v = x32_sum(v); acc[b][k] = v; }
    LAS float* red = (LAS float*)(L + M_RED);
    if (lane < 8) {
#pragma unroll
        for (int b = 0; b < 2; ++b) {
            *(LAS f32x4*)(red + (w * 2 + b) * 64 + lane * 8) = f32x4{acc[b][0], acc[b][1], acc[b][2], acc[b][3]};
            *(LAS f32x4*)(red + (w * 2 + b) * 64 + lane * 8 + 4) = f32x4{acc[b][4], acc[b][5], acc[b][6], acc[b][7]};
        }
    }
    bar();
    u32x2 r = {0u, 0u};
    if (tid < 32) {
        f32x4 s4 = {0.f, 0.f, 0.f, 0.f};
#pragma unroll
        for (int ww = 0; ww < 8; ++ww) s4 += *(LAS f32x4*)(red + (ww * 2 + (tid >> 4)) * 64 + (tid & 15) * 4);
        r.x = pk2(s4.x, s4.y); r.y = pk2(s4.z, s4.w);
    }
    return r;
}

DI void oproj_issue(const u16* W, int w, int lane) {
#pragma unroll
    for (int i = 0; i < 8; ++i) { VMCNTX(WIN); glds16(W + (size_t)(8 * w + i) * DM + 8 * lane, L_WO + w * 9216 + i * 1024); }
    VMCNTX(WIN);
    glds16(W + (size_t)(8 * w + (lane >> 3)) * DM + 512 + 8 * (lane & 7), L_WO + w * 9216 + 8192);
}
DI void oproj_finish(LAS unsigned char* L, rsrc_t rs, unsigned T, int prod, int w, int lane, int tid) {
    LAS float* o = (LAS float*)(L + M_O);
    float acc[2][8], ar[2][8];
#pragma unroll
    for (int k = 0; k < 8; ++k) { acc[0][k] = 0.f; acc[1][k] = 0.f; }
#pragma unroll
    for (int i = 0; i < 8; ++i) {
        const u32x4 v = *(LAS u32x4*)(L + L_WO + w * 9216 + i * 1024 + lane * 16);
        const float o0 = o[8 * w + i], o1 = o[64 + 8 * w + i];
#pragma unroll
        for (int k = 0; k < 4; ++k) {
            const float lo = lo2f(v[k]), hi = hi2f(v[k]);
            acc[0][2 * k] = fmaf(o0, lo, acc[0][2 * k]); acc[0][2 * k + 1] = fmaf(o0, hi, acc[0][2 * k + 1]);
            acc[1][2 * k] = fmaf(o1, lo, acc[1][2 * k]); acc[1][2 * k + 1] = fmaf(o1, hi, acc[1][2 * k + 1]);
        }
    }
    {
        const u32x4 v = *(LAS u32x4*)(L + L_WO + w * 9216 + 8192 + lane * 16);
        const float o0 = o[8 * w + (lane >> 3)], o1 = o[64 + 8 * w + (lane >> 3)];
#pragma unroll
        for (int k = 0; k < 4; ++k) {
            const float lo = lo2f(v[k]), hi = hi2f(v[k]);
            ar[0][2 * k] = o0 * lo; ar[0][2 * k + 1] = o0 * hi; ar[1][2 * k] = o1 * lo; ar[1][2 * k + 1] = o1 * hi;
        }
#pragma unroll
        for (int b = 0; b < 2; ++b)
#pragma unroll
            for (int k = 0; k < 8; ++k) { float t = ar[b][k]; t += dpp<XOR8>(t); t = x16_sum(t); t = x32_sum(t); ar[b][k] = t; }
    }
    LAS float* red = (LAS float*)(L + L_R0);
#pragma unroll
    for (int b = 0; b < 2; ++b) {
        *(LAS f32x4*)(red + (w * 2 + b) * DM + lane * 8) = f32x4{acc[b][0], acc[b][1], acc[b][2], acc[b][3]};
        *(LAS f32x4*)(red + (w * 2 + b) * DM + lane * 8 + 4) = f32x4{acc[b][4], acc[b][5], acc[b][6], acc[b][7]};
        if (lane < 8) {
            *(LAS f32x4*)(red + (w * 2 + b) * DM + 512 + lane * 8) = f32x4{ar[b][0], ar[b][1], ar[b][2], ar[b][3]};
            *(LAS f32x4*)(red + (w * 2 + b) * DM + 512 + lane * 8 + 4) = f32x4{ar[b][4], ar[b][5], ar[b][6], ar[b][7]};
        }
    }
    bar();
    if (tid < 288) {
        f32x4 s4 = {0.f, 0.f, 0.f, 0.f};
#pragma unroll
        for (int ww = 0; ww < 8; ++ww) s4 += *(LAS f32x4*)(red + ww * 2 * DM + tid * 4);
        const int o4 = tid * 4;
        const int off = OUTP_OFF + (o4 >> 4) * 16384 + prod * 128 + (o4 & 15) * 8;
        st_sc1(rs, off, u32x4{__float_as_uint(s4.x), T, __float_as_uint(s4.y), T});
        st_sc1(rs, off + 16, u32x4{__float_as_uint(s4.z), T, __float_as_uint(s4.w), T});
    }
}

struct AState { float m, l; float acc[2][8]; u32x4 q[2]; };
template <int S> DI void a_issue_k(u32x4 (&K)[3][4], const u16* kp) {
#pragma unroll
    for (int t = 0; t < 4; ++t) { VMCNTX(WIN); K[S][t] = ldnt(kp + t * 512); __builtin_amdgcn_sched_barrier(0); }
}
template <int S> DI void a_step(AState& a, u32x4 (&K)[3][4], u32x4 (&V)[3][4], const u16* kn, const u16* vn, bool more, int c) {
    float x[8];
#pragma unroll
    for (int t = 0; t < 4; ++t)
#pragma unroll
        for (int g = 0; g < 2; ++g) {
            float d = 0.f;
#pragma unroll
            for (int k = 0; k < 4; ++k) d = dot2(K[S][t][k], a.q[g][k], d);
            x[t * 2 + g] = d;
        }
    __builtin_amdgcn_sched_barrier(0);
    if (more) a_issue_k<S>(K, kn);
#define TR_STEP(HALF, CTRL, BIT) _Pragma("unroll") for (int h = 0; h < (HALF); ++h) { \
        const float lo_ = x[h], hi_ = x[h + (HALF)]; const float t1_ = lo_ + dpp<CTRL>(lo_), t2_ = hi_ + dpp<CTRL>(hi_); x[h] = (c & (BIT)) ? t2_ : t1_; }
    TR_STEP(4, XOR8, 8) TR_STEP(2, XOR7, 4) TR_STEP(1, XOR2, 2)
#undef TR_STEP
    const float sc = (x[0] + dpp<XOR1>(x[0])) * C2;
    float pm = sc; pm = fmaxf(pm, dpp<ROR4>(pm)); pm = fmaxf(pm, dpp<XOR8>(pm)); pm = x16_max(pm); pm = x32_max(pm);
    const float mn = fmaxf(a.m, pm);
    const float alpha = __builtin_amdgcn_exp2f(a.m - mn), pv = __builtin_amdgcn_exp2f(sc - mn);
    a.l = fmaf(a.l, alpha, pv); a.m = mn;
    const float al0 = dpp<BC + 0>(alpha), al1 = dpp<BC + 2>(alpha);
#pragma unroll
    for (int k = 0; k < 8; ++k) { a.acc[0][k] *= al0; a.acc[1][k] *= al1; }
    float pb[8];
    pb[0] = dpp<BC + 0>(pv); pb[1] = dpp<BC + 2>(pv); pb[2] = dpp<BC + 4>(pv); pb[3] = dpp<BC + 6>(pv);
    pb[4] = dpp<BC + 8>(pv); pb[5] = dpp<BC + 10>(pv); pb[6] = dpp<BC + 12>(pv); pb[7] = dpp<BC + 14>(pv);
#pragma unroll
    for (int t = 0; t < 4; ++t)
#pragma unroll
        for (int k = 0; k < 4; ++k) {
            const float lo = lo2f(V[S][t][k]), hi = hi2f(V[S][t][k]);
            a.acc[0][2 * k] = fmaf(pb[2 * t], lo, a.acc[0][2 * k]); a.acc[0][2 * k + 1] = fmaf(pb[2 * t], hi, a.acc[0][2 * k + 1]);
            a.acc[1][2 * k] = fmaf(pb[2 * t + 1], lo, a.acc[1][2 * k]); a.acc[1][2 * k + 1] = fmaf(pb[2 * t + 1], hi, a.acc[1][2 * k + 1]);
        }
    __builtin_amdgcn_sched_barrier(0);
    if (more) a_issue_k<S>(V, vn);
}

__global__ void __launch_bounds__(512, 2) fused_decode(Params p) {
    extern __shared__ __attribute__((aligned(16))) unsigned char smem[];
    LAS unsigned char* L = (LAS unsigned char*)smem;
    const int tid = threadIdx.x, lane = tid & 63, w = __builtin_amdgcn_readfirstlane(tid >> 6);
    const int pr = blockIdx.x >> 3, mem = blockIdx.x & 7;
    const int role = mem >> 1, j = mem & 1;
    const int b = pr >> 4, kvh = pr & 15, h = pr;
    const rsrc_t rs = __builtin_amdgcn_make_buffer_rsrc((void*)p.ws, 0, WS_END, 0x00020000);
    const unsigned long long t_start = __builtin_amdgcn_s_memrealtime();
    STAMP(0);
    const unsigned T = (unsigned)__builtin_amdgcn_readfirstlane((int)ld4_sc1(rs, EP_OFF)) + 1u;
    const int r4 = lane >> 4, c16 = lane & 15;

    if (role == 1) {
        QProj s;
        qproj_issue(s, p.x, p.wq + h * 128 + j * 64, w, lane);
        const u32x2 q = qproj_finish(s, L, w, lane, tid);
        if (tid < 32) st_sc1(rs, QA_OFF + (((tid >> 4) * 32 + h) * 64 + 32 * j + 2 * (tid & 15)) * 8, u32x4{q.x, T, q.y, T});
        STAMP(1);
        oproj_issue(p.wo + (size_t)(h * 128 + j * 64) * DM, w, lane);
    } else if (role == 0) {
        QProj s;
        qproj_issue(s, p.x, p.w_q + h * 128 + j * 64, w, lane);
        u32x4 uk[8];
#pragma unroll
        for (int t = 0; t < 8; ++t) uk[t] = ldnt(p.w_uk + (size_t)(256 * j + 32 * w + 4 * t + r4) * HD + h * 128 + 8 * c16);
        __builtin_amdgcn_sched_barrier(0);
        const u32x2 q = qproj_finish(s, L, w, lane, tid);
        LAS unsigned* qb = (LAS unsigned*)(L + M_QB);
        if (tid < 32) {
            st_sc1(rs, QBX_OFF + (((h * 2 + j) * 2 + (tid >> 4)) * 32 + 2 * (tid & 15)) * 8, u32x4{q.x, T, q.y, T});
            *(LAS u32x2*)(qb + (tid >> 4) * 64 + 32 * j + 2 * (tid & 15)) = q;
        }
        STAMP(1);
        if (w == 0) {
            const int l5 = lane & 31;
            u32x4 g;
            for (;;) {
                g = ld_sc1(rs, QBX_OFF + (((h * 2 + (j ^ 1)) * 2 + (l5 >> 4)) * 32 + 2 * (l5 & 15)) * 8);
                if (__all(g.y == T && g.w == T)) break;
                if (spin_over(t_start)) break;
            }
            if (lane < 32) *(LAS u32x2*)(qb + (l5 >> 4) * 64 + 32 * (j ^ 1) + 2 * (l5 & 15)) = u32x2{g.x, g.z};
        }
        bar();
        STAMP(2);
        const u32x4 q0 = *(LAS u32x4*)(qb + 4 * c16), q1 = *(LAS u32x4*)(qb + 64 + 4 * c16);
        float x[16];
#pragma unroll
        for (int t = 0; t < 8; ++t) {
            float d0 = 0.f, d1 = 0.f;
#pragma unroll
            for (int k = 0; k < 4; ++k) { d0 = dot2(uk[t][k], q0[k], d0); d1 = dot2(uk[t][k], q1[k], d1); }
            x[2 * t] = d0; x[2 * t + 1] = d1;
        }
#define TR_STEP(HALF, CTRL, BIT) _Pragma("unroll") for (int hh = 0; hh < (HALF); ++hh) { \
            const float lo_ = x[hh], hi_ = x[hh + (HALF)]; const float t1_ = lo_ + dpp<CTRL>(lo_), t2_ = hi_ + dpp<CTRL>(hi_); x[hh] = (c16 & (BIT)) ? t2_ : t1_; }
        TR_STEP(8, XOR8, 8) TR_STEP(4, XOR7, 4) TR_STEP(2, XOR2, 2) TR_STEP(1, XOR1, 1)
#undef TR_STEP
        LAS u16* qa = (LAS u16*)(L + M_QA);
        qa[(c16 & 1) * 256 + 32 * w + 4 * (c16 >> 1) + r4] = (u16)(pk2(x[0], 0.f) & 0xffffu);
        bar();
        if (w == 0) {
            const u32x4 v = *(LAS u32x4*)(qa + (lane >> 5) * 256 + 8 * (lane & 31));
            st_sc1(rs, QABS_OFF + (((lane >> 5) * 32 + h) * 512 + 256 * j + 8 * (lane & 31)) * 2, v);
            VMCNT(0);
            if (lane == 0) st4_sc1(rs, QABS_FLAG + (h * 2 + j) * 4, T);
        }
        STAMP(3);
#pragma unroll
        for (int t = 0; t < 8; ++t) { VMCNTX(WIN); glds16(p.w_uv + (size_t)(256 * j + 32 * w + 4 * t + r4) * HD + h * 128 + 8 * c16, L_R0 + (w * 8 + t) * 1024); }
        oproj_issue(p.w_o + (size_t)(h * 128 + j * 64) * DM, w, lane);
    } else if (role == 2) {
        const int idx = pr * 2 + j, bb = idx >> 5, sp = idx & 31;
#pragma unroll
        for (int i = 0; i < 16; ++i) {
            const int k = 16 * w + i;
            VMCNT(7);
            glds16(p.ckv + ((size_t)(bb * SK + 128 * sp + k) * 512 + ((unsigned)lane ^ fswz(k)) * 8), L_R0 + k * 1024);
        }
        if (w == 0) {
            for (;;) {
                const unsigned f = ld4_sc1(rs, QABS_FLAG + lane * 4);
                if (__all(f == T)) break;
                if (spin_over(t_start)) break;
            }
        }
        VMCNT(0);
        bar();
        STAMP(1);
        const int hf_w = w >> 2, wq4 = w & 3;
        u32x4 qv[4];
#pragma unroll
        for (int i = 0; i < 4; ++i) qv[i] = ld_sc1(rs, QABS_OFF + ((bb * 32 + 8 * wq4 + 2 * i + (lane >> 5)) * 512 + 256 * hf_w + 8 * (lane & 31)) * 2);
        f32x16 sacc;
#pragma unroll
        for (int i = 0; i < 16; ++i) sacc[i] = 0.f;
        const int hh = lane >> 5, l31 = lane & 31;
#pragma unroll
        for (int hf = 0; hf < 2; ++hf) {
            if (hf_w == hf) {
#pragma unroll
                for (int i = 0; i < 4; ++i) *(LAS u32x4*)(L + L_QB + (8 * wq4 + 2 * i + hh) * 528 + l31 * 16) = qv[i];
            }
            bar();
            if (w < 4) {
                const int row = 32 * w + l31;
                const unsigned f = fswz(row);
#pragma unroll
                for (int ks = 0; ks < 16; ++ks) {
                    const bf16x8 A = *(LAS bf16x8*)(L + L_R0 + row * 1024 + (((unsigned)(32 * hf + 2 * ks + hh)) ^ f) * 16);
                    const bf16x8 B = *(LAS bf16x8*)(L + L_QB + l31 * 528 + (2 * ks + hh) * 16);
                    sacc = __builtin_amdgcn_mfma_f32_32x32x16_bf16(A, B, sacc, 0, 0, 0);
                }
            }
            if (hf == 0) bar();
        }
        STAMP(2);
        LAS float* rm = (LAS float*)(L + M_RED);
        LAS float* rl = (LAS float*)(L + M_RED + 512);
        if (w < 4) {
            float mx = sacc[0];
#pragma unroll
            for (int i = 1; i < 16; ++i) mx = fmaxf(mx, sacc[i]);
            mx = x32_max(mx);
            if (lane < 32) rm[w * 32 + lane] = mx;
        }
        bar();
        const float M = fmaxf(fmaxf(rm[l31], rm[32 + l31]), fmaxf(rm[64 + l31], rm[96 + l31])) * C2;
        if (w < 4) {
            float ls = 0.f;
#pragma unroll
            for (int g = 0; g < 4; ++g) {
                float e[4];
#pragma unroll
                for (int i = 0; i < 4; ++i) { e[i] = __builtin_amdgcn_exp2f(fmaf(sacc[4 * g + i], C2, -M)); ls += e[i]; }
                *(LAS u32x2*)(L + L_QB + l31 * 272 + (32 * w + 8 * g + 4 * hh) * 2) = u32x2{pk2(e[0], e[1]), pk2(e[2], e[3])};
            }
            ls = x32_sum(ls);
            if (lane < 32) rl[w * 32 + lane] = ls;
        }
        bar();
        bf16x8 pf[8];
#pragma unroll
        for (int ks = 0; ks < 8; ++ks) pf[ks] = *(LAS bf16x8*)(L + L_QB + l31 * 272 + (16 * ks + 8 * hh) * 2);
        const int blk = (lane >> 4) & 1, q4 = (lane & 15) >> 2, p4 = lane & 3;
#pragma unroll
        for (int cti = 0; cti < 2; ++cti) {
            const int ct = 2 * w + cti;
            f32x16 o;
#pragma unroll
            for (int i = 0; i < 16; ++i) o[i] = 0.f;
#pragma unroll
            for (int ks = 0; ks < 8; ++ks) {
                const unsigned row0 = 16 * ks + 8 * hh + q4, row1 = row0 + 4;
                const unsigned ch = 4 * ct + 2 * blk + (p4 >> 1);
                const s16x4 lo = __builtin_amdgcn_ds_read_tr16_b64_v4i16((LAS s16x4*)(L + L_R0 + row0 * 1024 + (ch ^ fswz(row0)) * 16 + 8 * (p4 & 1)));
                const s16x4 hi = __builtin_amdgcn_ds_read_tr16_b64_v4i16((LAS s16x4*)(L + L_R0 + row1 * 1024 + (ch ^ fswz(row1)) * 16 + 8 * (p4 & 1)));
                const bf16x8 A = __builtin_shufflevector(lo, hi, 0, 1, 2, 3, 4, 5, 6, 7);
                o = __builtin_amdgcn_mfma_f32_32x32x16_bf16(A, pf[ks], o, 0, 0, 0);
            }
#pragma unroll
            for (int g = 0; g < 4; ++g)
                st_sc1(rs, BP_OFF + ((idx * 32 + l31) * 512 + 32 * ct + 8 * g + 4 * hh) * 4,
                       u32x4{__float_as_uint(o[4 * g]), __float_as_uint(o[4 * g + 1]), __float_as_uint(o[4 * g + 2]), __float_as_uint(o[4 * g + 3])});
        }
        if (tid < 32) st8_sc1(rs, BST_OFF + (idx * 32 + tid) * 8, u32x2{__float_as_uint(M), __float_as_uint(rl[tid] + rl[32 + tid] + rl[64 + tid] + rl[96 + tid])});
        VMCNT(0);
        bar();
        if (tid == 0) st4_sc1(rs, BP_FLAG + idx * 4, T);
        STAMP(3);
    }

    {
        const int n = role == 0 ? N_PB : role == 1 ? N_PA : role == 2 ? N_BA : N_PURE;
        const int base = 8 * (role == 0 ? j * N_PB : role == 1 ? 2 * N_PB + j * N_PA : role == 2 ? 2 * (N_PB + N_PA) + j * N_BA : 2 * (N_PB + N_PA + N_BA) + j * N_PURE);
        const size_t o0 = ((size_t)pr * SK + (size_t)(base + w) * 16) * 128 + lane * 8;
        const u16* kp = p.kc + o0;
        const u16* vp = p.vc + o0;
        constexpr int CS = 8 * 16 * 128;
        u32x4 K[3][4], V[3][4];
        a_issue_k<0>(K, kp); a_issue_k<0>(V, vp);
        if (n > 1) { a_issue_k<1>(K, kp + CS); a_issue_k<1>(V, vp + CS); }
        if (n > 2) { a_issue_k<2>(K, kp + 2 * CS); a_issue_k<2>(V, vp + 2 * CS); }
        AState a;
        {
            const int qo = QA_OFF + ((b * 32 + 2 * kvh) * 64 + 4 * c16) * 8;
            for (;;) {
                const u32x4 g0 = ld_sc1(rs, qo), g1 = ld_sc1(rs, qo + 16), g2 = ld_sc1(rs, qo + 512), g3 = ld_sc1(rs, qo + 528);
                a.q[0] = u32x4{g0.x, g0.z, g1.x, g1.z}; a.q[1] = u32x4{g2.x, g2.z, g3.x, g3.z};
                const bool ok = g0.y == T && g0.w == T && g1.y == T && g1.w == T && g2.y == T && g2.w == T && g3.y == T && g3.w == T;
                if (__all(ok)) break;
                if (spin_over(t_start)) break;
            }
        }
        STAMP(4);
        a.m = -INFINITY; a.l = 0.f;
#pragma unroll
        for (int k = 0; k < 8; ++k) { a.acc[0][k] = 0.f; a.acc[1][k] = 0.f; }
        for (int i = 0; i < n; i += 3) {
            a_step<0>(a, K, V, kp + (size_t)(i + 3) * CS, vp + (size_t)(i + 3) * CS, i + 3 < n, c16);
            if (i + 1 < n) a_step<1>(a, K, V, kp + (size_t)(i + 4) * CS, vp + (size_t)(i + 4) * CS, i + 4 < n, c16);
            if (i + 2 < n) a_step<2>(a, K, V, kp + (size_t)(i + 5) * CS, vp + (size_t)(i + 5) * CS, i + 5 < n, c16);
        }
        STAMP(5);
#pragma unroll
        for (int g = 0; g < 2; ++g)
#pragma unroll
            for (int k = 0; k < 8; ++k) a.acc[g][k] = x32_sum(x16_sum(a.acc[g][k]));
        float lt = a.l; lt += dpp<ROR4>(lt); lt += dpp<XOR8>(lt); lt = x32_sum(x16_sum(lt));
        LAS float* mrg = (LAS float*)(L + M_MRG);
        if (r4 == 0) {
#pragma unroll
            for (int g = 0; g < 2; ++g) {
                LAS float* rec = mrg + (w * 2 + g) * 132;
                *(LAS f32x4*)(rec + 4 + 8 * c16) = f32x4{a.acc[g][0], a.acc[g][1], a.acc[g][2], a.acc[g][3]};
                *(LAS f32x4*)(rec + 8 + 8 * c16) = f32x4{a.acc[g][4], a.acc[g][5], a.acc[g][6], a.acc[g][7]};
                if (c16 == 2 * g) { rec[0] = a.m; rec[1] = lt; }
            }
        }
        bar();
        if (tid < 128) {
            const int g = tid >> 6, d0 = 2 * (tid & 63);
            float M = -INFINITY;
#pragma unroll
            for (int ww = 0; ww < 8; ++ww) M = fmaxf(M, mrg[(ww * 2 + g) * 132]);
            float Ls = 0.f, o0 = 0.f, o1 = 0.f;
#pragma unroll
            for (int ww = 0; ww < 8; ++ww) {
                LAS float* rec = mrg + (ww * 2 + g) * 132;
                const float wt = __builtin_amdgcn_exp2f(rec[0] - M);
                Ls = fmaf(wt, rec[1], Ls); o0 = fmaf(wt, rec[4 + d0], o0); o1 = fmaf(wt, rec[5 + d0], o1);
            }
            const int hq = 2 * kvh + g;
            st_sc1(rs, AP_OFF + ((((hq * 2 + (d0 >> 6)) * 2 + b) * 8 + mem) * 64 + (d0 & 63)) * 8, u32x4{__float_as_uint(o0), T, __float_as_uint(o1), T});
            if ((tid & 63) == 0) st_sc1(rs, AST_OFF + ((hq * 2 + b) * 8 + mem) * 16, u32x4{__float_as_uint(M), T, __float_as_uint(Ls), T});
        }
        STAMP(6);
    }

    if (role == 0) {
        if (w == 0) {
            for (;;) {
                const unsigned f = ld4_sc1(rs, BP_FLAG + lane * 4);
                if (__all(f == T)) break;
                if (spin_over(t_start)) break;
            }
        }
        bar();
        STAMP(7);
        const int bb = w >> 2, s0 = 8 * (w & 3);
        const u32x2 st = ld8_sc1(rs, BST_OFF + ((bb * 32 + (lane & 31)) * 32 + h) * 8);
        u32x4 pd[8];
#pragma unroll
        for (int i = 0; i < 8; ++i) pd[i] = ld_sc1(rs, BP_OFF + (((bb * 32 + s0 + i) * 32 + h) * 512 + 256 * j + 4 * lane) * 4);
        const float ms = __uint_as_float(st.x), ls = __uint_as_float(st.y);
        float M = ms; M = fmaxf(M, dpp<XOR1>(M)); M = fmaxf(M, dpp<XOR2>(M)); M = fmaxf(M, dpp<ROR4>(M)); M = fmaxf(M, dpp<XOR8>(M)); M = x16_max(M);
        const float wt = __builtin_amdgcn_exp2f(ms - M);
        float Ls = wt * ls; Ls += dpp<XOR1>(Ls); Ls += dpp<XOR2>(Ls); Ls += dpp<ROR4>(Ls); Ls += dpp<XOR8>(Ls); Ls = x16_sum(Ls);
        const float wn = wt / Ls;
        f32x4 cc = {0.f, 0.f, 0.f, 0.f};
#pragma unroll
        for (int i = 0; i < 8; ++i) {
            const float wi = __builtin_bit_cast(float, __builtin_amdgcn_readlane(__builtin_bit_cast(int, wn), s0 + i));
            cc += wi * __builtin_bit_cast(f32x4, pd[i]);
        }
        LAS float* ccr = (LAS float*)(L + M_MRG);
        LAS float* ccs = (LAS float*)(L + M_RED);
        *(LAS f32x4*)(ccr + w * 256 + 4 * lane) = cc;
        bar();
        ccs[tid] = ccr[(tid >> 8) * 1024 + (tid & 255)] + ccr[(tid >> 8) * 1024 + 256 + (tid & 255)] + ccr[(tid >> 8) * 1024 + 512 + (tid & 255)] + ccr[(tid >> 8) * 1024 + 768 + (tid & 255)];
        VMCNT(0);
        bar();
        float acc[2][8];
#pragma unroll
        for (int k = 0; k < 8; ++k) { acc[0][k] = 0.f; acc[1][k] = 0.f; }
#pragma unroll
        for (int t = 0; t < 8; ++t) {
            const u32x4 v = *(LAS u32x4*)(L + L_R0 + (w * 8 + t) * 1024 + lane * 16);
            const float c0 = ccs[32 * w + 4 * t + r4], c1 = ccs[256 + 32 * w + 4 * t + r4];
#pragma unroll
            for (int k = 0; k < 4; ++k) {
                const float lo = lo2f(v[k]), hi = hi2f(v[k]);
                acc[0][2 * k] = fmaf(c0, lo, acc[0][2 * k]); acc[0][2 * k + 1] = fmaf(c0, hi, acc[0][2 * k + 1]);
                acc[1][2 * k] = fmaf(c1, lo, acc[1][2 * k]); acc[1][2 * k + 1] = fmaf(c1, hi, acc[1][2 * k + 1]);
            }
        }
#pragma unroll
        for (int g = 0; g < 2; ++g)
#pragma unroll
            for (int k = 0; k < 8; ++k) acc[g][k] = x32_sum(x16_sum(acc[g][k]));
        LAS float* red = (LAS float*)(L + M_MRG);
        if (r4 == 0) {
#pragma unroll
            for (int g = 0; g < 2; ++g) {
                *(LAS f32x4*)(red + (w * 2 + g) * 128 + 8 * c16) = f32x4{acc[g][0], acc[g][1], acc[g][2], acc[g][3]};
                *(LAS f32x4*)(red + (w * 2 + g) * 128 + 8 * c16 + 4) = f32x4{acc[g][4], acc[g][5], acc[g][6], acc[g][7]};
            }
        }
        bar();
        if (w == 0) {
            const int g = lane >> 5, dq = lane & 31;
            f32x4 s4 = {0.f, 0.f, 0.f, 0.f};
#pragma unroll
            for (int ww = 0; ww < 8; ++ww) s4 += *(LAS f32x4*)(red + (ww * 2 + g) * 128 + 4 * dq);
            const bool mine = (dq >> 4) == j;
            if (!mine) {
                const int off = CTX_OFF + (((h * 2 + (j ^ 1)) * 2 + g) * 64 + 4 * (dq & 15)) * 8;
                st_sc1(rs, off, u32x4{__float_as_uint(s4.x), T, __float_as_uint(s4.y), T});
                st_sc1(rs, off + 16, u32x4{__float_as_uint(s4.z), T, __float_as_uint(s4.w), T});
            }
            LAS float* own = (LAS float*)(L + M_QB);
            if (mine) *(LAS f32x4*)(own + g * 64 + 4 * (dq & 15)) = s4;
            u32x4 gr;
            for (;;) {
                gr = ld_sc1(rs, CTX_OFF + ((h * 2 + j) * 128 + 2 * lane) * 8);
                if (__all(gr.y == T && gr.w == T)) break;
                if (spin_over(t_start)) break;
            }
            asm volatile("s_waitcnt lgkmcnt(0)" ::: "memory");
            LAS float* o = (LAS float*)(L + M_O);
            o[2 * lane] = rbf(own[2 * lane] + __uint_as_float(gr.x));
            o[2 * lane + 1] = rbf(own[2 * lane + 1] + __uint_as_float(gr.z));
        }
        bar();
        STAMP(8);
        oproj_finish(L, rs, T, 64 + h * 2 + j, w, lane, tid);
        STAMP(9);
    } else if (role == 1) {
        const int bb = w >> 2, m0 = 2 * (w & 3);
        u32x4 gs, go;
        for (;;) {
            gs = ld_sc1(rs, AST_OFF + ((h * 2 + bb) * 8 + (lane & 7)) * 16);
            go = ld_sc1(rs, AP_OFF + ((((h * 2 + j) * 2 + bb) * 8 + m0) * 64 + 2 * lane) * 8);
            if (__all(gs.y == T && gs.w == T && go.y == T && go.w == T)) break;
            if (spin_over(t_start)) break;
        }
        STAMP(7);
        const float ms = __uint_as_float(gs.x), ls = __uint_as_float(gs.z);
        float M = ms; M = fmaxf(M, dpp<XOR1>(M)); M = fmaxf(M, dpp<XOR2>(M)); M = fmaxf(M, dpp<XOR7>(M));
        const float wt = __builtin_amdgcn_exp2f(ms - M);
        float Ls = wt * ls; Ls += dpp<XOR1>(Ls); Ls += dpp<XOR2>(Ls); Ls += dpp<XOR7>(Ls);
        const float wn = wt / Ls;
        const float w0 = __builtin_bit_cast(float, __builtin_amdgcn_readlane(__builtin_bit_cast(int, wn), m0));
        const float w1 = __builtin_bit_cast(float, __builtin_amdgcn_readlane(__builtin_bit_cast(int, wn), m0 + 1));
        const float wm = lane < 32 ? w0 : w1;
        const float v0 = x32_sum(wm * __uint_as_float(go.x)), v1 = x32_sum(wm * __uint_as_float(go.z));
        LAS float* red = (LAS float*)(L + M_RED);
        if (lane < 32) { red[w * 64 + 2 * lane] = v0; red[w * 64 + 2 * lane + 1] = v1; }
        VMCNT(0);
        bar();
        if (tid < 128) {
            const int g = tid >> 6, d = tid & 63;
            ((LAS float*)(L + M_O))[tid] = rbf(red[(4 * g) * 64 + d] + red[(4 * g + 1) * 64 + d] + red[(4 * g + 2) * 64 + d] + red[(4 * g + 3) * 64 + d]);
        }
        bar();
        STAMP(8);
        oproj_finish(L, rs, T, h * 2 + j, w, lane, tid);
        STAMP(9);
    }

    const int R = role == 3 ? pr * 2 + j : (role == 2 && pr < 4) ? 64 + pr * 2 + j : -1;
    if (R >= 0) {
        const int ob = R >= 36 ? 1 : 0;
        float gpart = 0.f;
        if (w == 0) {
#pragma unroll
            for (int i = 0; i < 9; ++i) gpart += rbf(bf2f(p.x[ob * DM + lane + 64 * i]) * bf2f(p.gw[lane + 64 * i]));
            gpart += dpp<XOR1>(gpart); gpart += dpp<XOR2>(gpart); gpart += dpp<ROR4>(gpart); gpart += dpp<XOR8>(gpart); gpart = x32_sum(x16_sum(gpart));
        }
        u32x4 g0, g1;
        for (;;) {
            g0 = ld_sc1(rs, OUTP_OFF + R * 16384 + w * 2048 + lane * 16);
            g1 = ld_sc1(rs, OUTP_OFF + R * 16384 + w * 2048 + 1024 + lane * 16);
            if (__all(g0.y == T && g0.w == T && g1.y == T && g1.w == T)) break;
            if (spin_over(t_start)) break;
        }
        float v0 = __uint_as_float(g0.x) + __uint_as_float(g1.x), v1 = __uint_as_float(g0.z) + __uint_as_float(g1.z);
        v0 += dpp<XOR8>(v0); v0 = x32_sum(x16_sum(v0));
        v1 += dpp<XOR8>(v1); v1 = x32_sum(x16_sum(v1));
        LAS float* fin = (LAS float*)(L + M_FIN);
        if (lane < 8) { fin[w * 16 + 2 * lane] = v0; fin[w * 16 + 2 * lane + 1] = v1; }
        bar();
        if (tid < 16) {
            const float sa = (fin[tid] + fin[16 + tid]) + (fin[32 + tid] + fin[48 + tid]);
            const float sb = (fin[64 + tid] + fin[80 + tid]) + (fin[96 + tid] + fin[112 + tid]);
            const float g = rbf(1.f / (1.f + __expf(-rbf(gpart))));
            const float res = rbf(g * rbf(sa)) + rbf(rbf(1.f - g) * rbf(sb));
            p.out[R * 16 + tid] = (u16)(pk2(res, 0.f) & 0xffffu);
            if (R == 0 && tid == 0) st4_sc1(rs, EP_OFF, T);
        }
        STAMP(10);
    }
}

extern "C" void kernel_launch(void* const* d_in, const int* in_sizes, int n_in, void* d_out, int out_size, void* d_ws, size_t ws_size, hipStream_t stream) {
    Params p;
    p.x = (const u16*)d_in[0]; p.kc = (const u16*)d_in[1]; p.vc = (const u16*)d_in[2]; p.wq = (const u16*)d_in[3]; p.wo = (const u16*)d_in[4];
    p.ckv = (const u16*)d_in[6]; p.w_q = (const u16*)d_in[7]; p.w_uk = (const u16*)d_in[8]; p.w_uv = (const u16*)d_in[9]; p.w_o = (const u16*)d_in[10];
    p.gw = (const u16*)d_in[11]; p.out = (u16*)d_out; p.ws = (unsigned char*)d_ws;
    (void)hipFuncSetAttribute((const void*)fused_decode, hipFuncAttributeMaxDynamicSharedMemorySize, LDS_BYTES);
    hipLaunchKernelGGL(fused_decode, dim3(256), dim3(512), LDS_BYTES, stream, p);
}
```

```cpp
#include <hip/hip_runtime.h>
#include <hip/hip_bf16.h>
#include <stdint.h>

typedef unsigned short u16;
typedef unsigned long long u64;
typedef unsigned u32x4 __attribute__((ext_vector_type(4)));
typedef unsigned u32x2 __attribute__((ext_vector_type(2)));
typedef float f32x4 __attribute__((ext_vector_type(4)));
typedef float f32x2 __attribute__((ext_vector_type(2)));
typedef float f32x16 __attribute__((ext_vector_type(16)));
typedef short s16x4 __attribute__((ext_vector_type(4)));
typedef short bf16x8 __attribute__((ext_vector_type(8)));
typedef __bf16 bf16x2 __attribute__((ext_vector_type(2)));
#define GAS __attribute__((address_space(1)))
#define LAS __attribute__((address_space(3)))
#define DI __device__ __forceinline__
#define RLX_AGENT __ATOMIC_RELAXED, __HIP_MEMORY_SCOPE_AGENT
#define RLX_WG __ATOMIC_RELAXED, __HIP_MEMORY_SCOPE_WORKGROUP

#define WIN 3
#define WINS 3
#define UKW 12
#define PSLEEP 4
#define NS_PA 4
#define NS_PB 1
constexpr int QLEN = 64 - 4 * NS_PB, NDYN = 4 * (QLEN - 8 * NS_PA);
static_assert(NS_PB == 1 || NS_PB == 0, "");

constexpr int DM = 576, HD = 4096, SK = 4096;
constexpr float C2 = 0.08838834764831845f * 1.4426950408889634f;

constexpr int EP_OFF = 0;
constexpr int TK_OFF = 4096;
constexpr int QABS_FLAG = 8192;
constexpr int BP_FLAG = QABS_FLAG + 16384;
constexpr int QA_OFF = BP_FLAG + 16384;
constexpr int QBX_OFF = QA_OFF + 131072;
constexpr int QABS_OFF = QBX_OFF + 131072;
constexpr int CTX_OFF = QABS_OFF + 131072;
constexpr int AST_OFF = CTX_OFF + 65536;
constexpr int BST_OFF = AST_OFF + 8192;
constexpr int AP_OFF = BST_OFF + 32768;
constexpr int OUTP_OFF = AP_OFF + 524288;
constexpr int BP_OFF = OUTP_OFF + 72 * 24576;
constexpr int WS_END = BP_OFF + 4194304;
constexpr int STAMP_OFF = 8 << 20;

constexpr int L_R0 = 0;
constexpr int L_WO = 65536;
constexpr int L_QB = 131072;
constexpr int M_CCR = 139264;
constexpr int L_M = 147968;
constexpr int M_RED = L_M;
constexpr int M_QB = L_M + 4096;
constexpr int M_QA = L_M + 4608;
constexpr int M_O = L_M + 5632;
constexpr int M_FIN = L_M + 5888;
constexpr int M_Q = L_M + 6400;
constexpr int M_MST = L_M + 6912;
constexpr int M_SYNC = L_M + 7040;
constexpr int M_MRG = L_M + 7168;
constexpr int LDS_BYTES = M_MRG + 8192;
static_assert(LDS_BYTES <= 163840, "LDS");
static_assert(WIN < 8, "the staged signal relies on it");

struct Params {
    const u16 *x, *kc, *vc, *wq, *wo, *ckv, *w_q, *w_uk, *w_uv, *w_o, *gw;
    u16* out;
    unsigned char* ws;
};

DI float bf2f(u16 v) { return __uint_as_float(((unsigned)v) << 16); }
DI float lo2f(unsigned v) { return __uint_as_float(v << 16); }
DI float hi2f(unsigned v) { return __uint_as_float(v & 0xffff0000u); }
DI unsigned pk2(float lo, float hi) { bf16x2 p; p[0] = (__bf16)lo; p[1] = (__bf16)hi; return __builtin_bit_cast(unsigned, p); }
DI float rbf(float f) { return (float)(__bf16)f; }
DI u32x4 ldnt(const u16* p) { return __builtin_nontemporal_load((const GAS u32x4*)p); }
DI float dot2(unsigned k, unsigned q, float acc) { return __builtin_amdgcn_fdot2_f32_bf16(__builtin_bit_cast(bf16x2, k), __builtin_bit_cast(bf16x2, q), acc, false); }
template <int CTRL> DI float dpp(float x) { return __builtin_bit_cast(float, __builtin_amdgcn_mov_dpp(__builtin_bit_cast(int, x), CTRL, 0xf, 0xf, true)); }
constexpr int XOR1 = 0xB1, XOR2 = 0x4E, XOR7 = 0x141, XOR8 = 0x128, ROR4 = 0x124, BC = 0x150;
DI float x32_red2(float a, float b) { auto s = __builtin_amdgcn_permlane32_swap(__float_as_uint(a), __float_as_uint(b), false, false); return __uint_as_float(s[0]) + __uint_as_float(s[1]); }
DI float x16_red2(float a, float b) { auto s = __builtin_amdgcn_permlane16_swap(__float_as_uint(a), __float_as_uint(b), false, false); return __uint_as_float(s[0]) + __uint_as_float(s[1]); }
DI float x16_sum(float x) { return x16_red2(x, x); }
DI float x32_sum(float x) { return x32_red2(x, x); }
DI float x16_max(float x) { auto s = __builtin_amdgcn_permlane16_swap(__float_as_uint(x), __float_as_uint(x), false, false); return fmaxf(__uint_as_float(s[0]), __uint_as_float(s[1])); }
DI float x32_max(float x) { auto s = __builtin_amdgcn_permlane32_swap(__float_as_uint(x), __float_as_uint(x), false, false); return fmaxf(__uint_as_float(s[0]), __uint_as_float(s[1])); }
#define VMCNT(n) asm volatile("s_waitcnt vmcnt(" #n ")" ::: "memory")
#define VMCNTX(n) VMCNT(n)
DI void glds16(const void* gsrc, unsigned lds_dst) {
    unsigned keep;
    asm volatile("s_mov_b32 %0, m0\n\ts_mov_b32 m0, %2\n\ts_nop 0\n\tglobal_load_lds_dwordx4 %1, off nt\n\ts_mov_b32 m0, %0" : "=&s"(keep) : "v"(gsrc), "s"(lds_dst) : "memory");
}
typedef __amdgpu_buffer_rsrc_t rsrc_t;
DI u32x4 ld_sc1(rsrc_t rs, int off) { return __builtin_amdgcn_raw_buffer_load_b128(rs, off, 0, 16); }
DI unsigned ld4_sc1(rsrc_t rs, int off) { return __builtin_amdgcn_raw_buffer_load_b32(rs, off, 0, 16); }
DI void st_sc1(rsrc_t rs, int off, u32x4 v) { __builtin_amdgcn_raw_buffer_store_b128(v, rs, off, 0, 16); }
DI u32x2 ld8_sc1(rsrc_t rs, int off) { return __builtin_amdgcn_raw_buffer_load_b64(rs, off, 0, 16); }
DI void st8_sc1(rsrc_t rs, int off, u32x2 v) { __builtin_amdgcn_raw_buffer_store_b64(v, rs, off, 0, 16); }
DI void st4_sc1(rsrc_t rs, int off, unsigned v) { __builtin_amdgcn_raw_buffer_store_b32(v, rs, off, 0, 16); }
DI u32x4 gran2(float a, float b, unsigned T) { return u32x4{__float_as_uint(a), T, __float_as_uint(b), T}; }
DI bool tagok(u32x4 g, unsigned T) { return g.y == T && g.w == T; }
DI bool spin_over(unsigned& budget) { __builtin_amdgcn_s_sleep(1); if (budget == 0u) return true; --budget; return false; }
DI unsigned fswz(unsigned row) { return ((row & 3u) << 2) | ((row >> 2) & 3u); }
DI unsigned get_tag(unsigned e) { asm volatile("" : "+v"(e)); return (unsigned)__builtin_amdgcn_readfirstlane((int)e) + 1u; }
DI s16x4 trd(LAS unsigned char* p) { return __builtin_amdgcn_ds_read_tr16_b64_v4i16((LAS s16x4*)p); }
DI f32x4 mfma16(bf16x8 a, bf16x8 b, f32x4 c) { return __builtin_amdgcn_mfma_f32_16x16x32_bf16(a, b, c, 0, 0, 0); }
DI bf16x8 asbf(u32x4 v) { return __builtin_bit_cast(bf16x8, v); }
DI void lds_signal(LAS unsigned* c, int lane) {
    asm volatile("s_waitcnt lgkmcnt(0)" ::: "memory");
    if (lane == 0) (void)__hip_atomic_fetch_add(c, 1u, RLX_WG);
}
template <int SLEEP = 0> DI void lds_wait(LAS unsigned* c, unsigned target, unsigned& t0) {
    unsigned n = 0;
    while ((unsigned)__builtin_amdgcn_readfirstlane((int)__hip_atomic_load(c, RLX_WG)) < target) {
        if (SLEEP) __builtin_amdgcn_s_sleep(SLEEP);
        if ((++n & 63u) == 0u && spin_over(t0)) break;
    }
    asm volatile("" ::: "memory");
}
DI void hbar() { asm volatile("s_waitcnt lgkmcnt(0)" ::: "memory"); __builtin_amdgcn_s_barrier(); asm volatile("" ::: "memory"); }
struct GBar { LAS unsigned* c; unsigned target; unsigned t0; int lane; };
#define GB_SLEEP 1
DI void gbar(GBar& g) { g.target += 8u; lds_signal(g.c, g.lane); lds_wait<GB_SLEEP>(g.c, g.target, g.t0); }

#define STP(i) ((GAS u64*)nullptr)
#define STAMP(i) do {} while (0)

template <int NL> struct QProj { u32x4 wv[NL]; u16 xs[2][NL]; };
template <int NL> DI void qproj_issue(QProj<NL>& s, const u16* x, const u16* W, int r0, int w, int lane) {
    const int gq = lane >> 3, ch = lane & 7;
#pragma unroll
    for (int i = 0; i < NL; ++i) { const int row = r0 + (w * NL + i) * 8 + gq; s.wv[i] = ldnt(W + (size_t)row * HD + ch * 8); }
    __builtin_amdgcn_sched_barrier(0);
#pragma unroll
    for (int i = 0; i < NL; ++i) { const int row = r0 + (w * NL + i) * 8 + gq; s.xs[0][i] = ((const GAS u16*)x)[row]; s.xs[1][i] = ((const GAS u16*)x)[DM + row]; }
    __builtin_amdgcn_sched_barrier(0);
}
template <int NL> DI void qproj_finish(QProj<NL>& s, LAS unsigned char* L, GBar& gb, bool hw, rsrc_t rs, unsigned T, int dst, int w, int lane) {
    float acc[2][8];
#pragma unroll
    for (int k = 0; k < 8; ++k) { acc[0][k] = 0.f; acc[1][k] = 0.f; }
#pragma unroll
    for (int i = 0; i < NL; ++i) {
        const float x0 = bf2f(s.xs[0][i]), x1 = bf2f(s.xs[1][i]);
#pragma unroll
        for (int k = 0; k < 4; ++k) {
            const float lo = lo2f(s.wv[i][k]), hi = hi2f(s.wv[i][k]);
            acc[0][2 * k] = fmaf(x0, lo, acc[0][2 * k]); acc[0][2 * k + 1] = fmaf(x0, hi, acc[0][2 * k + 1]);
            acc[1][2 * k] = fmaf(x1, lo, acc[1][2 * k]); acc[1][2 * k + 1] = fmaf(x1, hi, acc[1][2 * k + 1]);
        }
    }
    float r8[8], r4[4];
#pragma unroll
    for (int k = 0; k < 8; ++k) r8[k] = x32_red2(acc[0][k], acc[1][k]);
#pragma unroll
    for (int k = 0; k < 4; ++k) { r4[k] = x16_red2(r8[k], r8[k + 4]); r4[k] += dpp<XOR8>(r4[k]); }
    LAS float* red = (LAS float*)(L + M_RED);
    if (!(lane & 8)) *(LAS f32x4*)(red + (w * 2 + (lane >> 5)) * 64 + (lane & 7) * 8 + ((lane >> 4) & 1) * 4) = f32x4{r4[0], r4[1], r4[2], r4[3]};
    if (hw) hbar(); else gbar(gb);
    if (w == 0) {
        f32x2 s2 = {0.f, 0.f};
#pragma unroll
        for (int ww = 0; ww < 8; ++ww) s2 += *(LAS f32x2*)(red + ww * 128 + 2 * lane);
        st_sc1(rs, dst + ((lane >> 5) * 4096 + 2 * (lane & 31)) * 8, gran2(s2.x, s2.y, T));
    }
}
constexpr int KP0 = 5, KP1 = 4;

template <int KS> DI void oproj_issue(const u16* W, int w, int lane) {
#pragma unroll
    for (int i = 0; i < (36 * KS + 7) / 8; ++i) {
        const int pc = w + 8 * i;
        if (pc < 36 * KS) { VMCNTX(WINS); glds16(W + (size_t)pc * 512 + 8 * lane, L_WO + pc * 1024); }
    }
}
template <int KS> struct OFrag { bf16x8 f[5][KS]; };
template <int KS> DI void oproj_frags(OFrag<KS>& o, LAS unsigned char* L, int w, int lane) {
    const int g = lane >> 4, q4 = (lane & 15) >> 2, p4 = lane & 3;
#pragma unroll
    for (int i = 0; i < 5; ++i) {
        const int tt = (w + 8 * i < 36) ? w + 8 * i : 0;
#pragma unroll
        for (int ks = 0; ks < KS; ++ks) {
            LAS unsigned char* a = L + L_WO + (32 * ks + 8 * g + q4) * 1152 + tt * 32 + p4 * 8;
            const s16x4 lo = trd(a), hi = trd(a + 4 * 1152);
            o.f[i][ks] = __builtin_shufflevector(lo, hi, 0, 1, 2, 3, 4, 5, 6, 7);
        }
    }
}
template <int KS> DI void oproj_finish(const OFrag<KS>& o, LAS unsigned char* L, rsrc_t rs, unsigned T, int prod, int w, int lane) {
    bf16x8 bo[KS];
#pragma unroll
    for (int ks = 0; ks < KS; ++ks) bo[ks] = *(LAS bf16x8*)(L + M_O + (lane & 1) * 128 + 64 * ks + (lane >> 4) * 16);
#pragma unroll
    for (int i = 0; i < 5; ++i) {
        if (w + 8 * i < 36) {
            f32x4 d = {0.f, 0.f, 0.f, 0.f};
#pragma unroll
            for (int ks = 0; ks < KS; ++ks) d = mfma16(o.f[i][ks], bo[ks], d);
            if ((lane & 15) < 2) {
                const int off = OUTP_OFF + ((lane & 15) * 36 + w + 8 * i) * 24576 + prod * 128 + (lane >> 4) * 32;
                st_sc1(rs, off, gran2(d.x, d.y, T));
                st_sc1(rs, off + 16, gran2(d.z, d.w, T));
            }
        }
    }
}
template <int I, int N, int STRIDE> struct FetchStep {
    static DI unsigned missing(const u32x4 (&g)[N], unsigned T) { return (__all(tagok(g[I], T)) ? 0u : 1u << I) | FetchStep<I + 1, N, STRIDE>::missing(g, T); }
    static DI void again(u32x4 (&g)[N], rsrc_t rs, int off0, unsigned miss) { if ((miss >> I) & 1u) g[I] = ld_sc1(rs, off0 + I * STRIDE); FetchStep<I + 1, N, STRIDE>::again(g, rs, off0, miss); }
};
template <int N, int STRIDE> struct FetchStep<N, N, STRIDE> {
    static DI unsigned missing(const u32x4 (&)[N], unsigned) { return 0u; }
    static DI void again(u32x4 (&)[N], rsrc_t, int, unsigned) {}
};
template <int N, int STRIDE> DI void fetch_all(u32x4 (&g)[N], rsrc_t rs, int off0, unsigned T, unsigned& budget) {
#pragma unroll
    for (int i = 0; i < N; ++i) g[i] = ld_sc1(rs, off0 + i * STRIDE);
    for (;;) {
        const unsigned miss = FetchStep<0, N, STRIDE>::missing(g, T);
        if (miss == 0u || spin_over(budget)) break;
        FetchStep<0, N, STRIDE>::again(g, rs, off0, miss);
    }
}
DI void fetch_q(LAS unsigned char* L, rsrc_t rs, unsigned T, int b, int kvh, int w, int lane, unsigned& t0, GAS u64* stp) {
    LAS unsigned* flag = (LAS unsigned*)(L + M_SYNC) + 3;
    for (int i = 4; i < w; ++i) __builtin_amdgcn_s_sleep(16);
    const int o = QA_OFF + (b * 4096 + 256 * kvh + 4 * lane) * 8;
    for (;;) {
        const u32x4 a0 = ld_sc1(rs, o), a1 = ld_sc1(rs, o + 16), c0 = ld_sc1(rs, o + 65536), c1 = ld_sc1(rs, o + 65536 + 16);
        if (__all(tagok(a0, T) && tagok(a1, T) && tagok(c0, T) && tagok(c1, T))) {
            *(LAS u32x2*)(L + M_Q + lane * 8) = u32x2{pk2(__uint_as_float(a0.x) + __uint_as_float(c0.x), __uint_as_float(a0.z) + __uint_as_float(c0.z)),
                                                       pk2(__uint_as_float(a1.x) + __uint_as_float(c1.x), __uint_as_float(a1.z) + __uint_as_float(c1.z))};
            lds_signal(flag, lane);
            if (stp && lane == 0) *stp = __builtin_amdgcn_s_memrealtime();
            break;
        }
        if (__builtin_amdgcn_readfirstlane((int)__hip_atomic_load(flag, RLX_WG)) != 0) break;
        if (spin_over(t0)) { lds_signal(flag, lane); break; }
    }
}

struct AState { float m, l; f32x4 acc[4]; bf16x8 q[2]; };
template <int S, int KV> DI void a_issue(u32x4 (&K)[2][4], rsrc_t r, int voff, bool thr = true) {
#pragma unroll
    for (int t = 0; t < 4; ++t) {
        if (thr) VMCNTX(WIN);
        K[S][t] = __builtin_amdgcn_raw_buffer_load_b128(r, voff + (KV == 64 ? (t >> 1) * 2048 + (t & 1) * 128 : t * 256), 0, 2);
        __builtin_amdgcn_sched_barrier(0);
    }
}
struct Sched { int k, ns, cap, sbase; u64 tk; GAS u64* ctr; unsigned T; unsigned t0; };
DI int next_co(Sched& s, int lane) {
    int ch;
    if (s.k < s.ns) ch = s.sbase + 8 * s.k;
    else if (s.k >= s.cap) ch = -1;
    else {
        unsigned lo = (unsigned)__builtin_amdgcn_readfirstlane((int)(unsigned)s.tk), hi = (unsigned)__builtin_amdgcn_readfirstlane((int)(unsigned)(s.tk >> 32));
        while (hi != s.T) {
            if (spin_over(s.t0)) { lo = (unsigned)NDYN; break; }
            u64 t = 0;
            if (lane == 0) t = __hip_atomic_fetch_add(s.ctr, 1ull, RLX_AGENT);
            lo = (unsigned)__builtin_amdgcn_readfirstlane((int)(unsigned)t); hi = (unsigned)__builtin_amdgcn_readfirstlane((int)(unsigned)(t >> 32));
        }
        ch = lo < (unsigned)NDYN ? (int)((lo & 3u) * QLEN + 8 * NS_PA + (lo >> 2)) : -1;
    }
    ++s.k;
    if (ch < 0) s.cap = 0;
    else if (s.k >= s.ns && s.k < s.cap) { if (lane == 0) s.tk = __hip_atomic_fetch_add(s.ctr, 1ull, RLX_AGENT); }
    return ch >= 0 ? ch * 4096 : 0x40000000;
}
template <int S> DI bool a_step(AState& a, u32x4 (&K)[2][4], u32x4 (&V)[2][4], rsrc_t kb, rsrc_t vb, Sched& sc_, int lane, int ko, int vo) {
    f32x4 d0 = {0.f, 0.f, 0.f, 0.f}, d1 = {0.f, 0.f, 0.f, 0.f};
    d0 = mfma16(asbf(K[S][0]), a.q[0], d0); d1 = mfma16(asbf(K[S][2]), a.q[0], d1);
    d0 = mfma16(asbf(K[S][1]), a.q[1], d0); d1 = mfma16(asbf(K[S][3]), a.q[1], d1);
    asm volatile("" : "+v"(d0), "+v"(d1));
    const int co = next_co(sc_, lane);
    a_issue<S, 64>(K, kb, ko + co);
    f32x4 d;
#pragma unroll
    for (int k = 0; k < 4; ++k) {
        const auto sw = __builtin_amdgcn_permlane32_swap(__float_as_uint(d0[k]), __float_as_uint(d1[k]), false, false);
        d[k] = __uint_as_float(sw[0]) + dpp<0x104>(__uint_as_float(sw[1]));
    }
    float pm = fmaxf(fmaxf(d.x, d.y), fmaxf(d.z, d.w));
    pm = x32_max(x16_max(pm)) * C2;
    const float mn = fmaxf(a.m, pm);
    const float alpha = __builtin_amdgcn_exp2f(a.m - mn);
    const float p0 = __builtin_amdgcn_exp2f(fmaf(d.x, C2, -mn)), p1 = __builtin_amdgcn_exp2f(fmaf(d.y, C2, -mn));
    const float p2 = __builtin_amdgcn_exp2f(fmaf(d.z, C2, -mn)), p3 = __builtin_amdgcn_exp2f(fmaf(d.w, C2, -mn));
    a.l = fmaf(a.l, alpha, (p0 + p1) + (p2 + p3)); a.m = mn;
    const unsigned k01 = pk2(p0, p1), k23 = pk2(p2, p3);
    const bool up = (lane & 2) != 0;
    const bf16x8 A = asbf(u32x4{up ? 0u : k01, up ? 0u : k23, up ? k01 : 0u, up ? k23 : 0u});
    if (__any(alpha != 1.f)) {
        const float al0 = dpp<BC + 0>(alpha), al1 = dpp<BC + 1>(alpha);
#pragma unroll
        for (int k = 0; k < 4; ++k) { a.acc[k].x *= al0; a.acc[k].y *= al1; a.acc[k].z *= al0; a.acc[k].w *= al1; }
    }
#pragma unroll
    for (int k = 0; k < 4; ++k) {
        const u32x4 B = {__builtin_amdgcn_perm(V[S][1][k], V[S][0][k], 0x05040100u), __builtin_amdgcn_perm(V[S][3][k], V[S][2][k], 0x05040100u),
                         __builtin_amdgcn_perm(V[S][1][k], V[S][0][k], 0x07060302u), __builtin_amdgcn_perm(V[S][3][k], V[S][2][k], 0x07060302u)};
        a.acc[k] = mfma16(A, asbf(B), a.acc[k]);
    }
#pragma unroll
    for (int k = 0; k < 4; ++k) asm volatile("" : "+v"(a.acc[k]));
    a_issue<S, 256>(V, vb, vo + co);
    return co < 0x40000000;
}

__global__ void __launch_bounds__(1024, 4) fused_decode(Params p) {
    extern __shared__ __attribute__((aligned(16))) unsigned char smem[];
    LAS unsigned char* L = (LAS unsigned char*)smem;
    const int lane = threadIdx.x & 63, w16 = __builtin_amdgcn_readfirstlane(threadIdx.x >> 6);
    const int tid = threadIdx.x & 511, w = w16 & 7;
    const int pr = (blockIdx.x & 7) * 4 + ((blockIdx.x >> 3) & 3), mem = blockIdx.x >> 5;
    const int role = mem >> 1, j = mem & 1;
    const int b = pr >> 4, kvh = pr & 15, h = pr;
    const rsrc_t rs = __builtin_amdgcn_make_buffer_rsrc((void*)p.ws, 0, WS_END, 0x00020000);
    unsigned t_start = 1u << 17;
    const unsigned epoch = ld4_sc1(rs, EP_OFF);
    __builtin_amdgcn_sched_barrier(0);
    const int r4 = lane >> 4, c16 = lane & 15;
    LAS unsigned* sync = (LAS unsigned*)(L + M_SYNC);
    if (threadIdx.x < 8) sync[threadIdx.x] = 0u;
    const int cq = (role == 3 ? 2 : 0) + j;

    if (w16 < 8) {
        STAMP(0);
        const rsrc_t kb = __builtin_amdgcn_make_buffer_rsrc((void*)(p.kc + (size_t)pr * SK * 128), 0, SK * 256, 0x00020000);
        const rsrc_t vb = __builtin_amdgcn_make_buffer_rsrc((void*)(p.vc + (size_t)pr * SK * 128), 0, SK * 256, 0x00020000);
        const int ko = (lane & 7) * 256 + ((lane >> 3) & 1) * 64 + r4 * 16, vo = r4 * 1024 + c16 * 16;
        Sched sc_;
        sc_.k = 0; sc_.ns = (role & 1) ? NS_PA : role == 0 ? NS_PB : 0; sc_.cap = role == 0 ? NS_PB : 1 << 20;
        sc_.sbase = ((role & 1) ? QLEN * cq : 4 * QLEN + 8 * j) + w;
        sc_.ctr = (GAS u64*)(p.ws + TK_OFF + pr * 128);
        asm volatile("" : "+v"(sc_.ctr));
        sc_.tk = 0; sc_.t0 = t_start; sc_.T = 0;
        u32x4 K[2][4], V[2][4];
        unsigned T = 0u;
        int live = 0;
        hbar();
        if (role == 2) {
            hbar();
            const int idx = pr * 2 + j, bb = idx >> 5, sp = idx & 31;
#pragma unroll
            for (int i = 0; i < 16; ++i) {
                const int k = 16 * w + i;
                VMCNTX(WINS);
                glds16(p.ckv + ((size_t)(bb * SK + 128 * sp + k) * 512 + ((unsigned)lane ^ fswz(k)) * 8), L_R0 + k * 1024);
            }
            VMCNT(0);
            STAMP(14);
            T = get_tag(epoch); sc_.T = T;
#pragma unroll
            for (int i = 0; i < 8; ++i) hbar();
            if (lane == 0) sc_.tk = __hip_atomic_fetch_add(sc_.ctr, 1ull, RLX_AGENT);
            hbar();
        } else if (role == 0) {
            hbar(); hbar(); hbar(); hbar();
            T = get_tag(epoch); sc_.T = T;
#pragma unroll
            for (int t = 0; t < 8; ++t) {
                const int row = 32 * w + 4 * t + r4;
                VMCNTX(WINS);
                glds16(p.w_uv + (size_t)(256 * j + row) * HD + h * 128 + 8 * ((unsigned)c16 ^ fswz(row)), L_R0 + (w * 8 + t) * 1024);
            }
            oproj_issue<2>(p.w_o + (size_t)(h * 128 + j * 64) * DM, w, lane);
        }
        { const int co = next_co(sc_, lane); a_issue<0, 64>(K, kb, ko + co); a_issue<0, 256>(V, vb, vo + co); live += co < 0x40000000; }
        if (role & 1) {
            T = get_tag(epoch); sc_.T = T;
            if (mem == 6 && tid == 0) __hip_atomic_store(sc_.ctr, (u64)T << 32, RLX_AGENT);
            oproj_issue<1>(p.wo + (size_t)(h * 128 + cq * 32) * DM, w, lane);
        } else if (role == 0) {
            STAMP(14);
            hbar();
        }
        { const int co = next_co(sc_, lane); a_issue<1, 64>(K, kb, ko + co); a_issue<1, 256>(V, vb, vo + co); live += co < 0x40000000; }
        lds_signal(sync + 2, lane);
        STAMP(11);
        lds_wait<2>(sync + 3, 1u, t_start);
        AState a;
#pragma unroll
        for (int i = 0; i < 2; ++i) {
            const u32x4 q = *(LAS u32x4*)(L + M_Q + (lane & 1) * 256 + i * 128 + ((lane >> 2) & 1) * 64 + r4 * 16);
            a.q[i] = asbf(c16 < 8 ? q : u32x4{0u, 0u, 0u, 0u});
        }
        STAMP(4);
        a.m = -INFINITY; a.l = 0.f;
#pragma unroll
        for (int k = 0; k < 4; ++k) a.acc[k] = f32x4{0.f, 0.f, 0.f, 0.f};
        while (live > 0) {
            if (!a_step<0>(a, K, V, kb, vb, sc_, lane, ko, vo)) --live;
            if (live == 0) break;
            if (!a_step<1>(a, K, V, kb, vb, sc_, lane, ko, vo)) --live;
        }
        STAMP(5);
        const float lt = x32_sum(x16_sum(a.l));
        LAS float* mrg = (LAS float*)(L + M_MRG);
        LAS float* mst = (LAS float*)(L + M_MST);
        if (lane < 16) {
            *(LAS f32x4*)(mrg + (w * 2) * 128 + 8 * lane) = f32x4{a.acc[0].x, a.acc[0].z, a.acc[1].x, a.acc[1].z};
            *(LAS f32x4*)(mrg + (w * 2) * 128 + 8 * lane + 4) = f32x4{a.acc[2].x, a.acc[2].z, a.acc[3].x, a.acc[3].z};
            *(LAS f32x4*)(mrg + (w * 2 + 1) * 128 + 8 * lane) = f32x4{a.acc[0].y, a.acc[0].w, a.acc[1].y, a.acc[1].w};
            *(LAS f32x4*)(mrg + (w * 2 + 1) * 128 + 8 * lane + 4) = f32x4{a.acc[2].y, a.acc[2].w, a.acc[3].y, a.acc[3].w};
            if (lane < 2) *(LAS f32x2*)(mst + w * 4 + 2 * lane) = f32x2{a.m, lt};
        }
        lds_signal(sync + 1, lane);
        if (w < 2) {
            lds_wait(sync + 1, 16u, t_start);
            const int g = w, d0 = 2 * lane;
            float mw[8], M = -INFINITY;
#pragma unroll
            for (int ww = 0; ww < 8; ++ww) { mw[ww] = mst[ww * 4 + 2 * g]; M = fmaxf(M, mw[ww]); }
            const float Mr = M;
            M = M == -INFINITY ? 0.f : M;
            float Ls = 0.f, o0 = 0.f, o1 = 0.f;
#pragma unroll
            for (int ww = 0; ww < 8; ++ww) {
                const float wt = __builtin_amdgcn_exp2f(mw[ww] - M);
                const f32x2 v = *(LAS f32x2*)(mrg + (ww * 2 + g) * 128 + d0);
                Ls = fmaf(wt, mst[ww * 4 + 2 * g + 1], Ls); o0 = fmaf(wt, v.x, o0); o1 = fmaf(wt, v.y, o1);
            }
            const int hq = 2 * kvh + g;
            st_sc1(rs, AP_OFF + ((((hq * 4 + (d0 >> 5)) * 2 + b) * 8 + mem) * 32 + (d0 & 31)) * 8, gran2(o0, o1, T));
            if (lane == 0) st_sc1(rs, AST_OFF + ((hq * 2 + b) * 8 + mem) * 16, gran2(Mr, Ls, T));
        }
        STAMP(6);
        if (role == 0) { hbar(); hbar(); hbar(); hbar(); }
        return;
    }

    __builtin_amdgcn_s_setprio(2);
    GBar gb; gb.c = sync; gb.target = 0u; gb.t0 = t_start; gb.lane = lane;
    unsigned T;
    u32x4 uk[8];
    {
        const u16* W = ((role & 1) ? p.wq : p.w_q) + h * 128 + j * 64;
        const int dst = ((role & 1) ? QA_OFF : QBX_OFF) + (h * 128 + j * 64) * 8;
        unsigned tt0 = 0u, tt1 = 0u, tt2 = 0u;
        if (role < 2) {
            QProj<KP0> s;
            qproj_issue(s, p.x, W, 0, w, lane);
            if (w == 7) { tt0 = ld4_sc1(rs, lane * 65536); tt1 = ld4_sc1(rs, (64 + lane) * 65536); }
            hbar();
            if (role == 0) {
                VMCNTX(UKW);
#pragma unroll
                for (int t = 0; t < 8; ++t) uk[t] = ldnt(p.w_uk + (size_t)(256 * j + 32 * w + 4 * t + r4) * HD + h * 128 + 8 * c16);
                __builtin_amdgcn_sched_barrier(0);
            }
            T = get_tag(epoch);
            qproj_finish(s, L, gb, !(role & 1), rs, T, dst, w, lane);
        } else {
            QProj<KP1> s;
            qproj_issue(s, p.x, W, 64 * KP0, w, lane);
            if (w == 7) { tt0 = ld4_sc1(rs, lane * 65536); tt1 = ld4_sc1(rs, (64 + lane) * 65536); tt2 = ((const GAS unsigned*)p.out)[0]; }
            hbar();
            T = get_tag(epoch);
            qproj_finish(s, L, gb, !(role & 1), rs, T, dst + 65536, w, lane);
        }
        asm volatile("" :: "v"(tt0), "v"(tt1), "v"(tt2));
        lds_signal(sync + 1, lane);
        STAMP(1);
    }
    if (role & 1) {
        if (w >= 4) fetch_q(L, rs, T, b, kvh, w, lane, t_start, STP(12));
        lds_wait<8>(sync + 2, 8u, t_start);
        OFrag<1> of;
        oproj_frags(of, L, w, lane);
        lds_wait<8>(sync + 1, 16u, t_start);
        if (w < 4) {
            const int bb = w >> 1, dq = w & 1, mm = lane >> 3;
            u32x4 gs, go;
            for (;;) {
                gs = ld_sc1(rs, AST_OFF + ((h * 2 + bb) * 8 + mm) * 16);
                go = ld_sc1(rs, AP_OFF + ((((h * 4 + cq) * 2 + bb) * 8 + mm) * 32 + 16 * dq + 2 * (lane & 7)) * 8);
                if (__all(tagok(gs, T) && tagok(go, T))) break;
                if (spin_over(t_start)) break;
            }
            STAMP(7);
            const float ms = __uint_as_float(gs.x), ls = __uint_as_float(gs.z);
            float M = ms; M = fmaxf(M, dpp<XOR8>(M)); M = x32_max(x16_max(M));
            const float wt = __builtin_amdgcn_exp2f(ms - M);
            float Ls = wt * ls, v0 = wt * __uint_as_float(go.x), v1 = wt * __uint_as_float(go.z);
            Ls += dpp<XOR8>(Ls); v0 += dpp<XOR8>(v0); v1 += dpp<XOR8>(v1);
            Ls = x32_sum(x16_sum(Ls)); v0 = x32_sum(x16_sum(v0)); v1 = x32_sum(x16_sum(v1));
            const float inv = 1.f / Ls;
            if (lane < 8) *(LAS unsigned*)(L + M_O + bb * 128 + (16 * dq + 2 * lane) * 2) = pk2(v0 * inv, v1 * inv);
        }
        gbar(gb);
        STAMP(8);
        oproj_finish(of, L, rs, T, h * 4 + cq, w, lane);
        STAMP(9);
        return;
    }
    if (role == 0) {
        LAS unsigned* qb = (LAS unsigned*)(L + M_QB);
        if (w == 1) {
            const int o = QBX_OFF + ((lane >> 5) * 4096 + h * 128 + 4 * (lane & 31)) * 8;
            u32x4 a[2], c[2];
            fetch_all<2, 16>(a, rs, o, T, t_start);
            fetch_all<2, 16>(c, rs, o + 65536, T, t_start);
            *(LAS u32x2*)(qb + 2 * lane) = u32x2{pk2(__uint_as_float(a[0].x) + __uint_as_float(c[0].x), __uint_as_float(a[0].z) + __uint_as_float(c[0].z)),
                                                 pk2(__uint_as_float(a[1].x) + __uint_as_float(c[1].x), __uint_as_float(a[1].z) + __uint_as_float(c[1].z))};
        }
        hbar();
        STAMP(2);
        const u32x4 q0 = *(LAS u32x4*)(qb + 4 * c16), q1 = *(LAS u32x4*)(qb + 64 + 4 * c16);
        float x[16];
#pragma unroll
        for (int t = 0; t < 8; ++t) {
            float d0 = 0.f, d1 = 0.f;
#pragma unroll
            for (int k = 0; k < 4; ++k) { d0 = dot2(uk[t][k], q0[k], d0); d1 = dot2(uk[t][k], q1[k], d1); }
            x[2 * t] = d0; x[2 * t + 1] = d1;
        }
#define TR_STEP(HALF, CTRL, BIT) _Pragma("unroll") for (int hh = 0; hh < (HALF); ++hh) { \
            const float lo_ = x[hh], hi_ = x[hh + (HALF)]; const float t1_ = lo_ + dpp<CTRL>(lo_), t2_ = hi_ + dpp<CTRL>(hi_); x[hh] = (c16 & (BIT)) ? t2_ : t1_; }
        TR_STEP(8, XOR8, 8) TR_STEP(4, XOR7, 4) TR_STEP(2, XOR2, 2) TR_STEP(1, XOR1, 1)
#undef TR_STEP
        LAS u16* qa = (LAS u16*)(L + M_QA);
        qa[(c16 & 1) * 256 + 32 * w + 4 * (c16 >> 1) + r4] = (u16)(pk2(x[0], 0.f) & 0xffffu);
        hbar();
        if (w == 0) {
            st_sc1(rs, QABS_OFF + (((lane >> 5) * 32 + h) * 512 + 256 * j + 8 * (lane & 31)) * 2, *(LAS u32x4*)(qa + (lane >> 5) * 256 + 8 * (lane & 31)));
            VMCNT(0);
            st4_sc1(rs, QABS_FLAG + lane * 256 + (h * 2 + j) * 4, T);
        }
        STAMP(3);
        hbar();
        if (w >= 4) fetch_q(L, rs, T, b, kvh, w, lane, t_start, STP(12));
        hbar();
        OFrag<2> of;
        oproj_frags(of, L, w, lane);
        bf16x8 uf[8];
        {
            const int q4 = (lane & 15) >> 2, p4 = lane & 3;
#pragma unroll
            for (int ks = 0; ks < 8; ++ks) {
                const unsigned row0 = 32 * ks + 8 * r4 + q4, row1 = row0 + 4, ch = 2 * w + (p4 >> 1);
                const s16x4 lo = trd(L + L_R0 + row0 * 256 + (ch ^ fswz(row0)) * 16 + 8 * (p4 & 1));
                const s16x4 hi = trd(L + L_R0 + row1 * 256 + (ch ^ fswz(row1)) * 16 + 8 * (p4 & 1));
                uf[ks] = __builtin_shufflevector(lo, hi, 0, 1, 2, 3, 4, 5, 6, 7);
            }
        }
        const int bb = w >> 2, s0 = 8 * (w & 3);
        if (w == 0) {
            for (;;) {
                const unsigned f = ld4_sc1(rs, BP_FLAG + (h * 2 + j) * 256 + lane * 4);
                if (__all(f == T)) break;
                __builtin_amdgcn_s_sleep(PSLEEP);
                if (spin_over(t_start)) break;
            }
        }
        hbar();
        STAMP(7);
        const int hh = lane >> 5;
        const u32x2 st = ld8_sc1(rs, BST_OFF + ((bb * 32 + (lane & 31)) * 32 + h) * 8);
        u32x4 pd[4];
#pragma unroll
        for (int i = 0; i < 4; ++i) pd[i] = ld_sc1(rs, BP_OFF + ((bb * 32 + s0 + 2 * i + hh) * 32 + h) * 1024 + 512 * j + 16 * (lane & 31));
        const float ms = __uint_as_float(st.x), ls = __uint_as_float(st.y);
        float M = ms; M = fmaxf(M, dpp<XOR1>(M)); M = fmaxf(M, dpp<XOR2>(M)); M = fmaxf(M, dpp<ROR4>(M)); M = fmaxf(M, dpp<XOR8>(M)); M = x16_max(M);
        const float wt = __builtin_amdgcn_exp2f(ms - M);
        float Ls = wt * ls; Ls += dpp<XOR1>(Ls); Ls += dpp<XOR2>(Ls); Ls += dpp<ROR4>(Ls); Ls += dpp<XOR8>(Ls); Ls = x16_sum(Ls);
        const float wn = wt / Ls;
        float c8[8];
#pragma unroll
        for (int k = 0; k < 8; ++k) c8[k] = 0.f;
#pragma unroll
        for (int i = 0; i < 4; ++i) {
            const float wa = __builtin_bit_cast(float, __builtin_amdgcn_readlane(__builtin_bit_cast(int, wn), s0 + 2 * i));
            const float wb = __builtin_bit_cast(float, __builtin_amdgcn_readlane(__builtin_bit_cast(int, wn), s0 + 2 * i + 1));
            const float wi = hh ? wb : wa;
#pragma unroll
            for (int k = 0; k < 4; ++k) { c8[2 * k] = fmaf(wi, lo2f(pd[i][k]), c8[2 * k]); c8[2 * k + 1] = fmaf(wi, hi2f(pd[i][k]), c8[2 * k + 1]); }
        }
        f32x4 cc;
#pragma unroll
        for (int k = 0; k < 4; ++k) cc[k] = x32_red2(c8[k], c8[k + 4]);
        LAS float* ccr = (LAS float*)(L + M_CCR);
        *(LAS f32x4*)(ccr + w * 256 + 8 * (lane & 31) + 4 * hh) = cc;
        STAMP(13);
        hbar();
        STAMP(15);
        LAS u16* ccb = (LAS u16*)(L + M_RED);
        {
            const float v = (ccr[(tid >> 8) * 1024 + (tid & 255)] + ccr[(tid >> 8) * 1024 + 256 + (tid & 255)]) + (ccr[(tid >> 8) * 1024 + 512 + (tid & 255)] + ccr[(tid >> 8) * 1024 + 768 + (tid & 255)]);
            const float hi = rbf(v);
            const unsigned pk = pk2(hi, v - hi);
            ccb[tid] = (u16)(pk & 0xffffu); ccb[512 + tid] = (u16)(pk >> 16);
        }
        hbar();
        f32x4 d = {0.f, 0.f, 0.f, 0.f}, e = {0.f, 0.f, 0.f, 0.f};
#pragma unroll
        for (int ks = 0; ks < 8; ++ks) {
            const bf16x8 bh = *(LAS bf16x8*)(ccb + (lane & 1) * 256 + 32 * ks + 8 * r4);
            const bf16x8 bl = *(LAS bf16x8*)(ccb + 512 + (lane & 1) * 256 + 32 * ks + 8 * r4);
            d = mfma16(uf[ks], bh, d);
            e = mfma16(uf[ks], bl, e);
        }
        d += e;
        asm volatile("" : "+v"(d));
        STAMP(10);
        const int dl = 16 * (w & 3) + 4 * r4;
        if ((w >> 2) != j) {
            if (c16 < 2) {
                const int off = CTX_OFF + (((h * 2 + (j ^ 1)) * 2 + c16) * 64 + dl) * 8;
                st_sc1(rs, off, gran2(d.x, d.y, T));
                st_sc1(rs, off + 16, gran2(d.z, d.w, T));
            }
        } else {
            u32x4 g[2];
            fetch_all<2, 16>(g, rs, CTX_OFF + (((h * 2 + j) * 2 + (c16 & 1)) * 64 + dl) * 8, T, t_start);
            if (c16 < 2)
                *(LAS u32x2*)(L + M_O + c16 * 128 + dl * 2) = u32x2{pk2(d.x + __uint_as_float(g[0].x), d.y + __uint_as_float(g[0].z)), pk2(d.z + __uint_as_float(g[1].x), d.w + __uint_as_float(g[1].z))};
        }
        hbar();
        STAMP(8);
        oproj_finish(of, L, rs, T, 128 + h * 2 + j, w, lane);
        STAMP(9);
    } else {
        const int idx = pr * 2 + j, bb = idx >> 5;
        const int hf_w = w >> 2, wq4 = w & 3, hh = lane >> 5, l31 = lane & 31;
        if (w == 0) {
            for (;;) {
                const unsigned f = ld4_sc1(rs, QABS_FLAG + idx * 256 + lane * 4);
                if (__all(f == T)) break;
                __builtin_amdgcn_s_sleep(PSLEEP);
                if (spin_over(t_start)) break;
            }
        }
        hbar();
        STAMP(2);
        u32x4 qv[4];
#pragma unroll
        for (int i = 0; i < 4; ++i) qv[i] = ld_sc1(rs, QABS_OFF + ((bb * 32 + 8 * wq4 + 2 * i + hh) * 512 + 256 * hf_w + 8 * l31) * 2);
        f32x16 sacc;
#pragma unroll
        for (int i = 0; i < 16; ++i) sacc[i] = 0.f;
#pragma unroll
        for (int hf = 0; hf < 2; ++hf) {
            if (hf_w == hf) {
#pragma unroll
                for (int i = 0; i < 4; ++i) *(LAS u32x4*)(L + L_QB + (8 * wq4 + 2 * i + hh) * 528 + l31 * 16) = qv[i];
            }
            hbar();
            if (hf == 0) STAMP(9);
            if (w < 4) {
                const int row = 32 * w + l31;
                const unsigned f = fswz(row);
#pragma unroll
                for (int ks = 0; ks < 16; ++ks) {
                    const bf16x8 A = *(LAS bf16x8*)(L + L_R0 + row * 1024 + (((unsigned)(32 * hf + 2 * ks + hh)) ^ f) * 16);
                    const bf16x8 B = *(LAS bf16x8*)(L + L_QB + l31 * 528 + (2 * ks + hh) * 16);
                    sacc = __builtin_amdgcn_mfma_f32_32x32x16_bf16(A, B, sacc, 0, 0, 0);
                }
            }
            if (hf == 0) hbar();
        }
        LAS float* rm = (LAS float*)(L + M_RED);
        LAS float* rl = (LAS float*)(L + M_RED + 512);
        if (w < 4) {
            float mx = sacc[0];
#pragma unroll
            for (int i = 1; i < 16; ++i) mx = fmaxf(mx, sacc[i]);
            mx = x32_max(mx);
            if (lane < 32) rm[w * 32 + lane] = mx;
        }
        hbar();
        STAMP(12);
        const float M = fmaxf(fmaxf(rm[l31], rm[32 + l31]), fmaxf(rm[64 + l31], rm[96 + l31])) * C2;
        if (w < 4) {
            float ls = 0.f;
#pragma unroll
            for (int g = 0; g < 4; ++g) {
                float e[4];
#pragma unroll
                for (int i = 0; i < 4; ++i) { e[i] = __builtin_amdgcn_exp2f(fmaf(sacc[4 * g + i], C2, -M)); ls += e[i]; }
                *(LAS u32x2*)(L + L_QB + l31 * 272 + (32 * w + 8 * g + 4 * hh) * 2) = u32x2{pk2(e[0], e[1]), pk2(e[2], e[3])};
            }
            ls = x32_sum(ls);
            if (lane < 32) rl[w * 32 + lane] = ls;
        }
        hbar();
        STAMP(7);
        bf16x8 pf[8];
#pragma unroll
        for (int ks = 0; ks < 8; ++ks) pf[ks] = *(LAS bf16x8*)(L + L_QB + l31 * 272 + (16 * ks + 8 * hh) * 2);
        const int blk = (lane >> 4) & 1, q4 = (lane & 15) >> 2, p4 = lane & 3;
        u32x2 ob[2][4];
#pragma unroll
        for (int cti = 0; cti < 2; ++cti) {
            const int ct = 2 * w + cti;
            f32x16 o;
#pragma unroll
            for (int i = 0; i < 16; ++i) o[i] = 0.f;
#pragma unroll
            for (int ks = 0; ks < 8; ++ks) {
                const unsigned row0 = 16 * ks + 8 * hh + q4, row1 = row0 + 4;
                const unsigned ch = 4 * ct + 2 * blk + (p4 >> 1);
                const s16x4 lo = trd(L + L_R0 + row0 * 1024 + (ch ^ fswz(row0)) * 16 + 8 * (p4 & 1));
                const s16x4 hi = trd(L + L_R0 + row1 * 1024 + (ch ^ fswz(row1)) * 16 + 8 * (p4 & 1));
                const bf16x8 A = __builtin_shufflevector(lo, hi, 0, 1, 2, 3, 4, 5, 6, 7);
                o = __builtin_amdgcn_mfma_f32_32x32x16_bf16(A, pf[ks], o, 0, 0, 0);
            }
#pragma unroll
            for (int g = 0; g < 4; ++g) ob[cti][g] = u32x2{pk2(o[4 * g], o[4 * g + 1]), pk2(o[4 * g + 2], o[4 * g + 3])};
        }
        if (tid < 32) st8_sc1(rs, BST_OFF + (idx * 32 + tid) * 8, u32x2{__float_as_uint(M), __float_as_uint(rl[tid] + rl[32 + tid] + rl[64 + tid] + rl[96 + tid])});
        STAMP(8);
        hbar();
#pragma unroll
        for (int cti = 0; cti < 2; ++cti)
#pragma unroll
            for (int g = 0; g < 4; ++g) *(LAS u32x2*)(L + L_R0 + l31 * 1040 + (32 * (2 * w + cti) + 8 * g + 4 * hh) * 2) = ob[cti][g];
        hbar();
#pragma unroll
        for (int i = 0; i < 4; ++i) st_sc1(rs, BP_OFF + (idx * 32 + 4 * w + i) * 1024 + lane * 16, *(LAS u32x4*)(L + L_R0 + (4 * w + i) * 1040 + lane * 16));
        STAMP(13);
        VMCNT(0);
        hbar();
        if (w == 0) st4_sc1(rs, BP_FLAG + lane * 256 + idx * 4, T);
        STAMP(3);
        if (w >= 4) fetch_q(L, rs, T, b, kvh, w, lane, t_start, (GAS u64*)nullptr);
    }

    const int R = role == 2 ? pr * 2 + j : (pr < 4) ? 64 + pr * 2 + j : -1;
    if (R >= 0) {
        const int ob = R >= 36 ? 1 : 0;
        float gpart = 0.f;
        if (w == 0) {
#pragma unroll
            for (int i = 0; i < 9; ++i) gpart += rbf(bf2f(p.x[ob * DM + lane + 64 * i]) * bf2f(p.gw[lane + 64 * i]));
            gpart += dpp<XOR1>(gpart); gpart += dpp<XOR2>(gpart); gpart += dpp<ROR4>(gpart); gpart += dpp<XOR8>(gpart); gpart = x32_sum(x16_sum(gpart));
            gpart = rbf(1.f / (1.f + __expf(-rbf(gpart))));
        }
        lds_wait<8>(sync + 1, 16u, t_start);
        float v0, v1;
        if (w < 4) {
            for (;;) {
                const u32x4 gs = ld_sc1(rs, AST_OFF + ((2 * kvh * 2 + b) * 8 + (lane & 7)) * 16);
                if (__all(tagok(gs, T))) break;
                __builtin_amdgcn_s_sleep(4);
                if (spin_over(t_start)) break;
            }
            u32x4 g[4];
            fetch_all<4, 1024>(g, rs, OUTP_OFF + R * 24576 + w * 4096 + lane * 16, T, t_start);
            v0 = (__uint_as_float(g[0].x) + __uint_as_float(g[1].x)) + (__uint_as_float(g[2].x) + __uint_as_float(g[3].x));
            v1 = (__uint_as_float(g[0].z) + __uint_as_float(g[1].z)) + (__uint_as_float(g[2].z) + __uint_as_float(g[3].z));
        } else {
            for (;;) {
                const u32x4 gs = ld_sc1(rs, OUTP_OFF + R * 24576 + 16384 + (w - 4) * 2048 + (lane & 7) * 16);
                if (__all(tagok(gs, T))) break;
                __builtin_amdgcn_s_sleep(8);
                if (spin_over(t_start)) break;
            }
            u32x4 g[2];
            fetch_all<2, 1024>(g, rs, OUTP_OFF + R * 24576 + 16384 + (w - 4) * 2048 + lane * 16, T, t_start);
            v0 = __uint_as_float(g[0].x) + __uint_as_float(g[1].x);
            v1 = __uint_as_float(g[0].z) + __uint_as_float(g[1].z);
        }
        float v = x32_red2(v0, v1);
        v += dpp<XOR8>(v); v = x16_sum(v);
        LAS float* fin = (LAS float*)(L + M_FIN);
        if ((lane & 31) < 8) fin[w * 16 + 2 * (lane & 7) + (lane >> 5)] = v;
        gbar(gb);
        if (tid < 16) {
            const float sa = (fin[tid] + fin[16 + tid]) + (fin[32 + tid] + fin[48 + tid]);
            const float sb = (fin[64 + tid] + fin[80 + tid]) + (fin[96 + tid] + fin[112 + tid]);
            const float g = gpart;
            const float res = rbf(g * rbf(sa)) + rbf(rbf(1.f - g) * rbf(sb));
            p.out[R * 16 + tid] = (u16)(pk2(res, 0.f) & 0xffffu);
            if (R == 0 && tid == 0) st4_sc1(rs, EP_OFF, T);
        }
        if (role == 2) STAMP(10);
    }
}

extern "C" void kernel_launch(void* const* d_in, const int* in_sizes, int n_in, void* d_out, int out_size, void* d_ws, size_t ws_size, hipStream_t stream) {
    Params p;
    p.x = (const u16*)d_in[0]; p.kc = (const u16*)d_in[1]; p.vc = (const u16*)d_in[2]; p.wq = (const u16*)d_in[3]; p.wo = (const u16*)d_in[4];
    p.ckv = (const u16*)d_in[6]; p.w_q = (const u16*)d_in[7]; p.w_uk = (const u16*)d_in[8]; p.w_uv = (const u16*)d_in[9]; p.w_o = (const u16*)d_in[10];
    p.gw = (const u16*)d_in[11]; p.out = (u16*)d_out; p.ws = (unsigned char*)d_ws;
    (void)hipFuncSetAttribute((const void*)fused_decode, hipFuncAttributeMaxDynamicSharedMemorySize, LDS_BYTES);
    hipLaunchKernelGGL(fused_decode, dim3(256), dim3(1024), LDS_BYTES, stream, p);
}
```

```cpp
#include <hip/hip_runtime.h>
#include <hip/hip_bf16.h>
#include <stdint.h>

typedef unsigned short u16;
typedef unsigned long long u64;
typedef unsigned u32x4 __attribute__((ext_vector_type(4)));
typedef unsigned u32x2 __attribute__((ext_vector_type(2)));
typedef float f32x4 __attribute__((ext_vector_type(4)));
typedef float f32x2 __attribute__((ext_vector_type(2)));
typedef float f32x16 __attribute__((ext_vector_type(16)));
typedef short s16x4 __attribute__((ext_vector_type(4)));
typedef short bf16x8 __attribute__((ext_vector_type(8)));
typedef __bf16 bf16x2 __attribute__((ext_vector_type(2)));
#define GAS __attribute__((address_space(1)))
#define LAS __attribute__((address_space(3)))
#define DI __device__ __forceinline__
#define RLX_AGENT __ATOMIC_RELAXED, __HIP_MEMORY_SCOPE_AGENT
#define RLX_WG __ATOMIC_RELAXED, __HIP_MEMORY_SCOPE_WORKGROUP

#define WIN 7
#define WINS 3
#define CODE_KB 30
#define CSLEEP 16
#define UKW 12
#define PSLEEP 4
#define CH_PA 48
#define CH_PB 8
#define CH_BA 24
static_assert(4 * CH_PA + 2 * CH_PB + 2 * CH_BA == 256 && CH_PB >= 8 && CH_BA >= 8, "a pair has 256 chunks; every wave gets at least one");

constexpr int DM = 576, HD = 4096, SK = 4096;
constexpr float SCALE = 0.08838834764831845f, LOG2E = 1.4426950408889634f;
constexpr float C2 = SCALE * LOG2E;

constexpr int EP_OFF = 0;
constexpr int QABS_FLAG = 8192;
constexpr int BP_FLAG = QABS_FLAG + 16384;
constexpr int QA_OFF = BP_FLAG + 16384;
constexpr int QBX_OFF = QA_OFF + 131072;
constexpr int QABS_OFF = QBX_OFF + 131072;
constexpr int CTX_OFF = QABS_OFF + 131072;
constexpr int AST_OFF = CTX_OFF + 131072;
constexpr int BST_OFF = AST_OFF + 8192;
constexpr int AP_OFF = BST_OFF + 32768;
constexpr int OUTP_OFF = AP_OFF + 524288;
constexpr int BP_OFF = OUTP_OFF + 72 * 32768;
constexpr int WS_END = BP_OFF + 2097152;
static_assert(WS_END <= 128 * 65536, "the translation warm-up covers 8 MB");

constexpr int L_R0 = 0;
constexpr int L_WO = 65536;
constexpr int L_QB = 131072;
constexpr int M_CCR = 139264;
constexpr int L_M = 147968;
constexpr int M_RED = L_M;
constexpr int M_QB = L_M + 4096;
constexpr int M_QA = L_M + 4608;
constexpr int M_O = L_M + 5632;
constexpr int M_FIN = L_M + 5888;
constexpr int M_Q = L_M + 6400;
constexpr int M_MST = L_M + 6912;
constexpr int M_SYNC = L_M + 7040;
constexpr int M_MRG = L_M + 7168;
constexpr int LDS_BYTES = M_MRG + 8192;
static_assert(LDS_BYTES <= 163840, "LDS");
static_assert(WIN < 8, "the staged signal relies on it");

struct Params {
    const u16 *x, *kc, *vc, *wq, *wo, *ckv, *w_q, *w_uk, *w_uv, *w_o, *gw;
    u16* out;
    unsigned char* ws;
};

DI float bf2f(u16 v) { return __uint_as_float(((unsigned)v) << 16); }
DI float lo2f(unsigned v) { return __uint_as_float(v << 16); }
DI float hi2f(unsigned v) { return __uint_as_float(v & 0xffff0000u); }
DI unsigned pk2(float lo, float hi) { bf16x2 p; p[0] = (__bf16)lo; p[1] = (__bf16)hi; return __builtin_bit_cast(unsigned, p); }
DI float rbf(float f) { return (float)(__bf16)f; }
DI u32x4 ldnt(const u16* p) { return __builtin_nontemporal_load((const GAS u32x4*)p); }
DI float dot2(unsigned k, unsigned q, float acc) { return __builtin_amdgcn_fdot2_f32_bf16(__builtin_bit_cast(bf16x2, k), __builtin_bit_cast(bf16x2, q), acc, false); }
template <int CTRL> DI float dpp(float x) { return __builtin_bit_cast(float, __builtin_amdgcn_mov_dpp(__builtin_bit_cast(int, x), CTRL, 0xf, 0xf, true)); }
constexpr int XOR1 = 0xB1, XOR2 = 0x4E, XOR7 = 0x141, XOR8 = 0x128, ROR4 = 0x124, BC = 0x150;
DI float x32_red2(float a, float b) { auto s = __builtin_amdgcn_permlane32_swap(__float_as_uint(a), __float_as_uint(b), false, false); return __uint_as_float(s[0]) + __uint_as_float(s[1]); }
DI float x16_red2(float a, float b) { auto s = __builtin_amdgcn_permlane16_swap(__float_as_uint(a), __float_as_uint(b), false, false); return __uint_as_float(s[0]) + __uint_as_float(s[1]); }
DI float x16_sum(float x) { return x16_red2(x, x); }
DI float x32_sum(float x) { return x32_red2(x, x); }
DI float x16_max(float x) { auto s = __builtin_amdgcn_permlane16_swap(__float_as_uint(x), __float_as_uint(x), false, false); return fmaxf(__uint_as_float(s[0]), __uint_as_float(s[1])); }
DI float x32_max(float x) { auto s = __builtin_amdgcn_permlane32_swap(__float_as_uint(x), __float_as_uint(x), false, false); return fmaxf(__uint_as_float(s[0]), __uint_as_float(s[1])); }
#define VMCNT(n) asm volatile("s_waitcnt vmcnt(" #n ")" ::: "memory")
#define VMCNTX(n) VMCNT(n)
DI void glds16(const void* gsrc, unsigned lds_dst) {
    unsigned keep;
    asm volatile("s_mov_b32 %0, m0\n\ts_mov_b32 m0, %2\n\ts_nop 0\n\tglobal_load_lds_dwordx4 %1, off nt\n\ts_mov_b32 m0, %0" : "=&s"(keep) : "v"(gsrc), "s"(lds_dst) : "memory");
}
DI void glds16_keep(const void* gsrc, unsigned lds_dst) {
    unsigned keep;
    asm volatile("s_mov_b32 %0, m0\n\ts_mov_b32 m0, %2\n\ts_nop 0\n\tglobal_load_lds_dwordx4 %1, off\n\ts_mov_b32 m0, %0" : "=&s"(keep) : "v"(gsrc), "s"(lds_dst) : "memory");
}
typedef __amdgpu_buffer_rsrc_t rsrc_t;
DI u32x4 ld_sc1(rsrc_t rs, int off) { return __builtin_amdgcn_raw_buffer_load_b128(rs, off, 0, 16); }
DI unsigned ld4_sc1(rsrc_t rs, int off) { return __builtin_amdgcn_raw_buffer_load_b32(rs, off, 0, 16); }
DI void st_sc1(rsrc_t rs, int off, u32x4 v) { __builtin_amdgcn_raw_buffer_store_b128(v, rs, off, 0, 16); }
DI u32x2 ld8_sc1(rsrc_t rs, int off) { return __builtin_amdgcn_raw_buffer_load_b64(rs, off, 0, 16); }
DI void st8_sc1(rsrc_t rs, int off, u32x2 v) { __builtin_amdgcn_raw_buffer_store_b64(v, rs, off, 0, 16); }
DI void st4_sc1(rsrc_t rs, int off, unsigned v) { __builtin_amdgcn_raw_buffer_store_b32(v, rs, off, 0, 16); }
DI u32x4 gran2(float a, float b, unsigned T) { return u32x4{__float_as_uint(a), T, __float_as_uint(b), T}; }
DI bool tagok(u32x4 g, unsigned T) { return g.y == T && g.w == T; }
DI bool spin_over(unsigned& budget) { __builtin_amdgcn_s_sleep(1); if (budget == 0u) return true; --budget; return false; }
DI unsigned fswz(unsigned row) { return ((row & 3u) << 2) | ((row >> 2) & 3u); }
DI unsigned get_tag(unsigned e) { asm volatile("" : "+v"(e)); return (unsigned)__builtin_amdgcn_readfirstlane((int)e) + 1u; }
DI s16x4 trd(LAS unsigned char* p) { return __builtin_amdgcn_ds_read_tr16_b64_v4i16((LAS s16x4*)p); }
DI f32x4 mfma16(bf16x8 a, bf16x8 b, f32x4 c) { return __builtin_amdgcn_mfma_f32_16x16x32_bf16(a, b, c, 0, 0, 0); }
DI bf16x8 asbf(u32x4 v) { return __builtin_bit_cast(bf16x8, v); }
DI void lds_signal(LAS unsigned* c, int lane) {
    asm volatile("s_waitcnt lgkmcnt(0)" ::: "memory");
    if (lane == 0) (void)__hip_atomic_fetch_add(c, 1u, RLX_WG);
}
template <int SLEEP = 0> DI void lds_wait(LAS unsigned* c, unsigned target, unsigned& t0) {
    unsigned n = 0;
    while ((unsigned)__builtin_amdgcn_readfirstlane((int)__hip_atomic_load(c, RLX_WG)) < target) {
        if (SLEEP) __builtin_amdgcn_s_sleep(SLEEP);
        if ((++n & 7u) == 0u && spin_over(t0)) break;
    }
    asm volatile("" ::: "memory");
}
DI void hbar() { asm volatile("s_waitcnt lgkmcnt(0)" ::: "memory"); __builtin_amdgcn_s_barrier(); asm volatile("" ::: "memory"); }
struct GBar { LAS unsigned* c; unsigned target; unsigned t0; int lane; };
#define GB_SLEEP 1
DI void gbar8(GBar& g) { g.target += 8u; lds_signal(g.c, g.lane); lds_wait<8>(g.c, g.target, g.t0); }
DI void gbar(GBar& g) { g.target += 8u; lds_signal(g.c, g.lane); lds_wait<GB_SLEEP>(g.c, g.target, g.t0); }

template <int NL> struct QProj { u32x4 wv[NL]; u16 xs[2][NL]; };
template <int NL> DI void qproj_issue(QProj<NL>& s, const u16* x, const u16* W, int r0, int w, int lane) {
    const int gq = lane >> 3, ch = lane & 7;
#pragma unroll
    for (int i = 0; i < NL; ++i) { const int row = r0 + (w * NL + i) * 8 + gq; s.wv[i] = ldnt(W + (size_t)row * HD + ch * 8); }
    __builtin_amdgcn_sched_barrier(0);
#pragma unroll
    for (int i = 0; i < NL; ++i) { const int row = r0 + (w * NL + i) * 8 + gq; s.xs[0][i] = ((const GAS u16*)x)[row]; s.xs[1][i] = ((const GAS u16*)x)[DM + row]; }
    __builtin_amdgcn_sched_barrier(0);
}
template <int NL> DI void qproj_finish(QProj<NL>& s, LAS unsigned char* L, GBar& gb, bool hw, rsrc_t rs, unsigned T, int dst, int w, int lane) {
    float acc[2][8];
#pragma unroll
    for (int k = 0; k < 8; ++k) { acc[0][k] = 0.f; acc[1][k] = 0.f; }
#pragma unroll
    for (int i = 0; i < NL; ++i) {
        const float x0 = bf2f(s.xs[0][i]), x1 = bf2f(s.xs[1][i]);
#pragma unroll
        for (int k = 0; k < 4; ++k) {
            const float lo = lo2f(s.wv[i][k]), hi = hi2f(s.wv[i][k]);
            acc[0][2 * k] = fmaf(x0, lo, acc[0][2 * k]); acc[0][2 * k + 1] = fmaf(x0, hi, acc[0][2 * k + 1]);
            acc[1][2 * k] = fmaf(x1, lo, acc[1][2 * k]); acc[1][2 * k + 1] = fmaf(x1, hi, acc[1][2 * k + 1]);
        }
    }
    float r8[8], r4[4];
#pragma unroll
    for (int k = 0; k < 8; ++k) r8[k] = x32_red2(acc[0][k], acc[1][k]);
#pragma unroll
    for (int k = 0; k < 4; ++k) { r4[k] = x16_red2(r8[k], r8[k + 4]); r4[k] += dpp<XOR8>(r4[k]); }
    LAS float* red = (LAS float*)(L + M_RED);
    if (!(lane & 8)) *(LAS f32x4*)(red + (w * 2 + (lane >> 5)) * 64 + (lane & 7) * 8 + ((lane >> 4) & 1) * 4) = f32x4{r4[0], r4[1], r4[2], r4[3]};
    if (hw) hbar(); else gbar(gb);
    if (w == 0) {
        f32x2 s2 = {0.f, 0.f};
#pragma unroll
        for (int ww = 0; ww < 8; ++ww) s2 += *(LAS f32x2*)(red + ww * 128 + 2 * lane);
        st_sc1(rs, dst + ((lane >> 5) * 4096 + 2 * (lane & 31)) * 8, gran2(s2.x, s2.y, T));
    }
}
constexpr int KP0 = 5, KP1 = 4;

template <int KS> DI void oproj_issue(const u16* W, int lbase, int w, int lane) {
#pragma unroll
    for (int i = 0; i < (36 * KS + 7) / 8; ++i) {
        const int pc = w + 8 * i;
        if (pc < 36 * KS) { VMCNTX(WINS); glds16(W + (size_t)pc * 512 + 8 * lane, lbase + pc * 1024); }
    }
}
template <int KS> struct OFrag { bf16x8 f[5][KS]; };
template <int KS> DI void oproj_frags(OFrag<KS>& o, LAS unsigned char* L, int lbase, int w, int lane) {
    const int g = lane >> 4, q4 = (lane & 15) >> 2, p4 = lane & 3;
#pragma unroll
    for (int i = 0; i < 5; ++i) {
        const int tt = (w + 8 * i < 36) ? w + 8 * i : 0;
#pragma unroll
        for (int ks = 0; ks < KS; ++ks) {
            LAS unsigned char* a = L + lbase + (32 * ks + 8 * g + q4) * 1152 + tt * 32 + p4 * 8;
            const s16x4 lo = trd(a), hi = trd(a + 4 * 1152);
            o.f[i][ks] = __builtin_shufflevector(lo, hi, 0, 1, 2, 3, 4, 5, 6, 7);
        }
    }
}
template <int KS> DI void oproj_finish(const OFrag<KS>& o, LAS unsigned char* L, int mo, rsrc_t rs, unsigned T, int prod, int w, int lane) {
    bf16x8 bo[KS];
#pragma unroll
    for (int ks = 0; ks < KS; ++ks) bo[ks] = *(LAS bf16x8*)(L + mo + (lane & 1) * 128 + 64 * ks + (lane >> 4) * 16);
#pragma unroll
    for (int i = 0; i < 5; ++i) {
        if (w + 8 * i < 36) {
            f32x4 d = {0.f, 0.f, 0.f, 0.f};
#pragma unroll
            for (int ks = 0; ks < KS; ++ks) d = mfma16(bo[ks], o.f[i][ks], d);
            if (lane < 16) {
                const int off = OUTP_OFF + (w + 8 * i) * 32768 + prod * 128 + lane * 8;
                st8_sc1(rs, off, u32x2{__float_as_uint(d.x), T});
                st8_sc1(rs, off + 36 * 32768, u32x2{__float_as_uint(d.y), T});
            }
        }
    }
}
template <int I, int N, int STRIDE> struct FetchStep {
    static DI unsigned missing(const u32x4 (&g)[N], unsigned T) { return (__all(tagok(g[I], T)) ? 0u : 1u << I) | FetchStep<I + 1, N, STRIDE>::missing(g, T); }
    static DI void again(u32x4 (&g)[N], rsrc_t rs, int off0, unsigned miss) { if ((miss >> I) & 1u) g[I] = ld_sc1(rs, off0 + I * STRIDE); FetchStep<I + 1, N, STRIDE>::again(g, rs, off0, miss); }
};
template <int N, int STRIDE> struct FetchStep<N, N, STRIDE> {
    static DI unsigned missing(const u32x4 (&)[N], unsigned) { return 0u; }
    static DI void again(u32x4 (&)[N], rsrc_t, int, unsigned) {}
};
template <int N, int STRIDE> DI void fetch_all(u32x4 (&g)[N], rsrc_t rs, int off0, unsigned T, unsigned& budget) {
#pragma unroll
    for (int i = 0; i < N; ++i) g[i] = ld_sc1(rs, off0 + i * STRIDE);
    for (;;) {
        const unsigned miss = FetchStep<0, N, STRIDE>::missing(g, T);
        if (miss == 0u || spin_over(budget)) break;
        FetchStep<0, N, STRIDE>::again(g, rs, off0, miss);
    }
}
DI void fetch_q(LAS unsigned char* L, rsrc_t rs, unsigned T, int b, int kvh, int w, int lane, unsigned& t0) {
    LAS unsigned* flag = (LAS unsigned*)(L + M_SYNC) + 3;
    for (int i = 4; i < w; ++i) __builtin_amdgcn_s_sleep(16);
    const int o = QA_OFF + (b * 4096 + 256 * kvh + 4 * lane) * 8;
    for (;;) {
        const u32x4 a0 = ld_sc1(rs, o), a1 = ld_sc1(rs, o + 16), c0 = ld_sc1(rs, o + 65536), c1 = ld_sc1(rs, o + 65536 + 16);
        if (__all(tagok(a0, T) && tagok(a1, T) && tagok(c0, T) && tagok(c1, T))) {
            *(LAS u32x2*)(L + M_Q + lane * 8) = u32x2{pk2(__uint_as_float(a0.x) + __uint_as_float(c0.x), __uint_as_float(a0.z) + __uint_as_float(c0.z)),
                                                       pk2(__uint_as_float(a1.x) + __uint_as_float(c1.x), __uint_as_float(a1.z) + __uint_as_float(c1.z))};
            lds_signal(flag, lane);
            break;
        }
        if (__builtin_amdgcn_readfirstlane((int)__hip_atomic_load(flag, RLX_WG)) != 0) break;
        if (spin_over(t0)) { lds_signal(flag, lane); break; }
    }
}

struct AState { float m, l; f32x4 acc[4]; bf16x8 q; };
template <int S> DI void a_issue(u32x4 (&K)[2][4], rsrc_t r, int voff, bool thr = true) {
#pragma unroll
    for (int t = 0; t < 4; ++t) {
        if (thr) VMCNTX(WIN);
        K[S][t] = __builtin_amdgcn_raw_buffer_load_b128(r, voff + t * 1024, 0, 2);
        __builtin_amdgcn_sched_barrier(0);
    }
}
struct Sched { int left, co; };
DI int next_co(Sched& s) {
    const int co = s.left > 0 ? s.co : 0x40000000;
    --s.left; s.co += 32768;
    return co;
}
template <int S> DI bool a_step(AState& a, u32x4 (&K)[2][4], u32x4 (&V)[2][4], rsrc_t kb, rsrc_t vb, Sched& sc_, int lane, int ko, int vo) {
    f32x4 dd[4];
#pragma unroll
    for (int t = 0; t < 4; ++t) dd[t] = mfma16(asbf(K[S][t]), a.q, f32x4{0.f, 0.f, 0.f, 0.f});
    asm volatile("" : "+v"(dd[0]), "+v"(dd[1]), "+v"(dd[2]), "+v"(dd[3]));
    const int co = next_co(sc_);
    a_issue<S>(K, kb, ko + co);
    f32x4 d;
#pragma unroll
    for (int t = 0; t < 4; ++t) {
        const float lo = (lane & 4) ? dd[t].y : dd[t].x, hi = (lane & 4) ? dd[t].w : dd[t].z;
        float v = (lane & 8) ? hi : lo;
        v += dpp<ROR4>(v); v += dpp<XOR8>(v);
        d[t] = v;
    }
    {
        const unsigned r01 = pk2(d.x, d.y), r23 = pk2(d.z, d.w);
        const unsigned s01 = pk2(lo2f(r01) * SCALE, hi2f(r01) * SCALE), s23 = pk2(lo2f(r23) * SCALE, hi2f(r23) * SCALE);
        d = f32x4{lo2f(s01), hi2f(s01), lo2f(s23), hi2f(s23)};
    }
    float pm = fmaxf(fmaxf(d.x, d.y), fmaxf(d.z, d.w));
    pm = x32_max(x16_max(pm)) * LOG2E;
    const float mn = fmaxf(a.m, pm);
    const float alpha = __builtin_amdgcn_exp2f(a.m - mn);
    const float p0 = __builtin_amdgcn_exp2f(fmaf(d.x, LOG2E, -mn)), p1 = __builtin_amdgcn_exp2f(fmaf(d.y, LOG2E, -mn));
    const float p2 = __builtin_amdgcn_exp2f(fmaf(d.z, LOG2E, -mn)), p3 = __builtin_amdgcn_exp2f(fmaf(d.w, LOG2E, -mn));
    a.l = fmaf(a.l, alpha, (p0 + p1) + (p2 + p3)); a.m = mn;
    const unsigned k01 = pk2(p0, p1), k23 = pk2(p2, p3);
    const bool up = (lane & 2) != 0;
    const bf16x8 A = asbf(u32x4{up ? 0u : k01, up ? 0u : k23, up ? k01 : 0u, up ? k23 : 0u});
    if (__any(alpha != 1.f)) {
        const float al0 = dpp<BC + 0>(alpha), al1 = dpp<BC + 1>(alpha);
#pragma unroll
        for (int k = 0; k < 4; ++k) { a.acc[k].x *= al0; a.acc[k].y *= al1; a.acc[k].z *= al0; a.acc[k].w *= al1; }
    }
#pragma unroll
    for (int k = 0; k < 4; ++k) {
        const u32x4 B = {__builtin_amdgcn_perm(V[S][1][k], V[S][0][k], 0x05040100u), __builtin_amdgcn_perm(V[S][3][k], V[S][2][k], 0x05040100u),
                         __builtin_amdgcn_perm(V[S][1][k], V[S][0][k], 0x07060302u), __builtin_amdgcn_perm(V[S][3][k], V[S][2][k], 0x07060302u)};
        a.acc[k] = mfma16(A, asbf(B), a.acc[k]);
    }
#pragma unroll
    for (int k = 0; k < 4; ++k) asm volatile("" : "+v"(a.acc[k]));
    a_issue<S>(V, vb, vo + co);
    return co < 0x40000000;
}

__global__ void __launch_bounds__(1024, 4) fused_decode(Params p) {
    extern __shared__ __attribute__((aligned(16))) unsigned char smem[];
    LAS unsigned char* L = (LAS unsigned char*)smem;
    const int lane = threadIdx.x & 63, w16 = __builtin_amdgcn_readfirstlane(threadIdx.x >> 6);
    if (w16 < 2) {
        u64 pc;
        asm volatile("s_getpc_b64 %0" : "=s"(pc));
        const int kb = (blockIdx.x >> 3) * 2 + w16;
        if (kb < CODE_KB) glds16_keep((const void*)((pc & ~63ull) + (u64)kb * 1024 + lane * 16), M_MRG + w16 * 1024);
    }
    const int tid = threadIdx.x & 511, w = w16 & 7;
    const int pr = (blockIdx.x & 7) * 4 + ((blockIdx.x >> 3) & 3), mem = blockIdx.x >> 5;
    const int role = mem >> 1, j = mem & 1;
    const int b = pr >> 4, kvh = pr & 15, h = pr;
    const rsrc_t rs = __builtin_amdgcn_make_buffer_rsrc((void*)p.ws, 0, WS_END, 0x00020000);
    unsigned t_start = 1u << 17;
    const unsigned epoch = ld4_sc1(rs, EP_OFF);
    __builtin_amdgcn_sched_barrier(0);
    const int r4 = lane >> 4, c16 = lane & 15;
    LAS unsigned* sync = (LAS unsigned*)(L + M_SYNC);
    if (threadIdx.x < 8) sync[threadIdx.x] = 0u;
    const int cq = (role == 3 ? 2 : 0) + j;

    if (w16 < 8) {
        const rsrc_t kb = __builtin_amdgcn_make_buffer_rsrc((void*)(p.kc + (size_t)pr * SK * 128), 0, SK * 256, 0x00020000);
        const rsrc_t vb = __builtin_amdgcn_make_buffer_rsrc((void*)(p.vc + (size_t)pr * SK * 128), 0, SK * 256, 0x00020000);
        const int ko = ((lane >> 2) & 3) * 256 + r4 * 64 + (lane & 3) * 16, vo = lane * 16;
        Sched sc_;
        sc_.left = (((role & 1) ? CH_PA : role == 0 ? CH_PB : CH_BA) + 7 - w) >> 3;
        sc_.co = (((role & 1) ? CH_PA * cq : role == 0 ? 4 * CH_PA + CH_PB * j : 4 * CH_PA + 2 * CH_PB + CH_BA * j) + w) * 4096;
        u32x4 K[2][4], V[2][4];
        unsigned T = 0u;
        int live = 0;
        hbar();
        if (role == 2) {
            hbar();
            const int idx = pr * 2 + j, bb = idx >> 5, sp = idx & 31;
#pragma unroll
            for (int i = 0; i < 16; ++i) {
                const int k = 16 * w + i;
                VMCNTX(WINS);
                glds16(p.ckv + ((size_t)(bb * SK + 128 * sp + k) * 512 + ((unsigned)lane ^ fswz(k)) * 8), L_R0 + k * 1024);
            }
        } else if (role == 0) {
            hbar(); hbar(); hbar(); hbar();
            T = get_tag(epoch);
#pragma unroll
            for (int t = 0; t < 8; ++t) {
                const int row = 32 * w + 4 * t + r4;
                VMCNTX(WINS);
                glds16(p.w_uv + (size_t)(256 * j + row) * HD + h * 128 + 8 * ((unsigned)c16 ^ fswz(row)), L_R0 + (w * 8 + t) * 1024);
            }
        }
        { const int co = next_co(sc_); a_issue<0>(K, kb, ko + co); a_issue<0>(V, vb, vo + co); live += co < 0x40000000; }
        if (role & 1) {
            T = get_tag(epoch);
            oproj_issue<1>(p.w_o + (size_t)(h * 128 + cq * 32) * DM, L_R0, w, lane);
            oproj_issue<1>(p.wo + (size_t)(h * 128 + cq * 32) * DM, L_WO, w, lane);
        } else if (role == 0) {
            hbar();
        } else {
            T = get_tag(epoch);
#pragma unroll
            for (int i = 0; i < 9; ++i) hbar();
        }
        { const int co = next_co(sc_); a_issue<1>(K, kb, ko + co); a_issue<1>(V, vb, vo + co); live += co < 0x40000000; }
        lds_signal(sync + 2, lane);
        lds_wait<2>(sync + 3, 1u, t_start);
        AState a;
        a.q = *(LAS bf16x8*)(L + M_Q + (lane & 1) * 256 + (4 * r4 + (c16 >> 2)) * 16);
        a.m = -INFINITY; a.l = 0.f;
#pragma unroll
        for (int k = 0; k < 4; ++k) a.acc[k] = f32x4{0.f, 0.f, 0.f, 0.f};
        while (live > 0) {
            if (!a_step<0>(a, K, V, kb, vb, sc_, lane, ko, vo)) --live;
            if (live == 0) break;
            if (!a_step<1>(a, K, V, kb, vb, sc_, lane, ko, vo)) --live;
        }
        const float lt = x32_sum(x16_sum(a.l));
        LAS float* mrg = (LAS float*)(L + M_MRG);
        LAS float* mst = (LAS float*)(L + M_MST);
        if (lane < 16) {
            *(LAS f32x4*)(mrg + (w * 2) * 128 + 8 * lane) = f32x4{a.acc[0].x, a.acc[0].z, a.acc[1].x, a.acc[1].z};
            *(LAS f32x4*)(mrg + (w * 2) * 128 + 8 * lane + 4) = f32x4{a.acc[2].x, a.acc[2].z, a.acc[3].x, a.acc[3].z};
            *(LAS f32x4*)(mrg + (w * 2 + 1) * 128 + 8 * lane) = f32x4{a.acc[0].y, a.acc[0].w, a.acc[1].y, a.acc[1].w};
            *(LAS f32x4*)(mrg + (w * 2 + 1) * 128 + 8 * lane + 4) = f32x4{a.acc[2].y, a.acc[2].w, a.acc[3].y, a.acc[3].w};
            if (lane < 2) *(LAS f32x2*)(mst + w * 4 + 2 * lane) = f32x2{a.m, lt};
        }
        lds_signal(sync + 1, lane);
        if (w < 2) {
            lds_wait(sync + 1, 16u, t_start);
            const int g = w, d0 = 2 * lane;
            float mw[8], M = -INFINITY;
#pragma unroll
            for (int ww = 0; ww < 8; ++ww) { mw[ww] = mst[ww * 4 + 2 * g]; M = fmaxf(M, mw[ww]); }
            const float Mr = M;
            float Ls = 0.f, o0 = 0.f, o1 = 0.f;
#pragma unroll
            for (int ww = 0; ww < 8; ++ww) {
                const float wt = __builtin_amdgcn_exp2f(mw[ww] - M);
                const f32x2 v = *(LAS f32x2*)(mrg + (ww * 2 + g) * 128 + d0);
                Ls = fmaf(wt, mst[ww * 4 + 2 * g + 1], Ls); o0 = fmaf(wt, v.x, o0); o1 = fmaf(wt, v.y, o1);
            }
            const int hq = 2 * kvh + g;
            st_sc1(rs, AP_OFF + ((((hq * 4 + (d0 >> 5)) * 2 + b) * 8 + mem) * 32 + (d0 & 31)) * 8, gran2(o0, o1, T));
            if (lane == 0) st_sc1(rs, AST_OFF + ((hq * 2 + b) * 8 + mem) * 16, gran2(Mr, Ls, T));
        }
        if (role == 0) { hbar(); hbar(); hbar(); }
        return;
    }

    __builtin_amdgcn_s_setprio(2);
    GBar gb; gb.c = sync; gb.target = 0u; gb.t0 = t_start; gb.lane = lane;
    unsigned T;
    if (role != 0) {
        const u16* W = ((role & 1) ? p.wq : p.w_q) + h * 128 + j * 64;
        const int dst = ((role & 1) ? QA_OFF : QBX_OFF) + (h * 128 + j * 64) * 8;
        unsigned tt0 = 0u, tt1 = 0u, tt2 = 0u;
        if (role == 1) {
            QProj<KP0> s;
            qproj_issue(s, p.x, W, 0, w, lane);
            if (w == 7) { tt0 = ld4_sc1(rs, lane * 65536); tt1 = ld4_sc1(rs, (64 + lane) * 65536); }
            hbar();
            T = get_tag(epoch);
            qproj_finish(s, L, gb, false, rs, T, dst, w, lane);
        } else {
            QProj<KP1> s;
            qproj_issue(s, p.x, W, 64 * KP0, w, lane);
            if (w == 7) { tt0 = ld4_sc1(rs, lane * 65536); tt1 = ld4_sc1(rs, (64 + lane) * 65536); tt2 = ((const GAS unsigned*)p.out)[0]; }
            hbar();
            T = get_tag(epoch);
            qproj_finish(s, L, gb, role == 2, rs, T, dst + 65536, w, lane);
        }
        asm volatile("" :: "v"(tt0), "v"(tt1), "v"(tt2));
        lds_signal(sync + 1, lane);
    }
    if (role & 1) {
        if (w >= 4) fetch_q(L, rs, T, b, kvh, w, lane, t_start);
        lds_wait<8>(sync + 2, 8u, t_start);
        {
            OFrag<1> ofb;
            oproj_frags(ofb, L, L_R0, w, lane);
            if (w == 0) {
                u32x4 g;
                for (;;) {
                    g = ld_sc1(rs, CTX_OFF + ((h * 4 + cq) * 128 + 2 * lane) * 8);
                    if (__all(tagok(g, T))) break;
                    __builtin_amdgcn_s_sleep(CSLEEP);
                    if (spin_over(t_start)) break;
                }
                const float v0 = x32_sum(__uint_as_float(g.x)), v1 = x32_sum(__uint_as_float(g.z));
                if (lane < 32) *(LAS unsigned*)(L + M_O + 64 + (lane >> 4) * 128 + 4 * (lane & 15)) = pk2(v0, v1);
            }
            gbar8(gb);
            oproj_finish(ofb, L, M_O + 64, rs, T, 128 + h * 4 + cq, w, lane);
        }
        OFrag<1> of;
        oproj_frags(of, L, L_WO, w, lane);
        lds_wait<8>(sync + 1, 16u, t_start);
        if (w < 4) {
            const int bb = w >> 1, dq = w & 1, mm = lane >> 3;
            u32x4 gs, go;
            for (;;) {
                gs = ld_sc1(rs, AST_OFF + ((h * 2 + bb) * 8 + mm) * 16);
                go = ld_sc1(rs, AP_OFF + ((((h * 4 + cq) * 2 + bb) * 8 + mm) * 32 + 16 * dq + 2 * (lane & 7)) * 8);
                if (__all(tagok(gs, T) && tagok(go, T))) break;
                if (spin_over(t_start)) break;
            }
            const float ms = __uint_as_float(gs.x), ls = __uint_as_float(gs.z);
            float M = ms; M = fmaxf(M, dpp<XOR8>(M)); M = x32_max(x16_max(M));
            const float wt = __builtin_amdgcn_exp2f(ms - M);
            float Ls = wt * ls, v0 = wt * __uint_as_float(go.x), v1 = wt * __uint_as_float(go.z);
            Ls += dpp<XOR8>(Ls); v0 += dpp<XOR8>(v0); v1 += dpp<XOR8>(v1);
            Ls = x32_sum(x16_sum(Ls)); v0 = x32_sum(x16_sum(v0)); v1 = x32_sum(x16_sum(v1));
            const float inv = 1.f / Ls;
            if (lane < 8) *(LAS unsigned*)(L + M_O + bb * 128 + (16 * dq + 2 * lane) * 2) = pk2(v0 * inv, v1 * inv);
        }
        gbar(gb);
        oproj_finish(of, L, M_O, rs, T, h * 4 + cq, w, lane);
        return;
    }
    if (role == 0) {
        u32x4 uk[8];
        {
            unsigned tt0 = 0u, tt1 = 0u, tt2 = 0u;
            QProj<KP0> s;
            qproj_issue(s, p.x, p.w_q + h * 128 + j * 64, 0, w, lane);
            if (w == 7) { tt0 = ld4_sc1(rs, lane * 65536); tt1 = ld4_sc1(rs, (64 + lane) * 65536); tt2 = ((const GAS unsigned*)p.out)[0]; }
            hbar();
            VMCNTX(UKW);
#pragma unroll
            for (int t = 0; t < 8; ++t) uk[t] = ldnt(p.w_uk + (size_t)(256 * j + 32 * w + 4 * t + r4) * HD + h * 128 + 8 * c16);
            __builtin_amdgcn_sched_barrier(0);
            T = get_tag(epoch);
            qproj_finish(s, L, gb, true, rs, T, QBX_OFF + (h * 128 + j * 64) * 8, w, lane);
            asm volatile("" :: "v"(tt0), "v"(tt1), "v"(tt2));
            lds_signal(sync + 1, lane);
        }
        LAS unsigned* qb = (LAS unsigned*)(L + M_QB);
        if (w == 1) {
            const int o = QBX_OFF + ((lane >> 5) * 4096 + h * 128 + 4 * (lane & 31)) * 8;
            u32x4 a[2], c[2];
            fetch_all<2, 16>(a, rs, o, T, t_start);
            fetch_all<2, 16>(c, rs, o + 65536, T, t_start);
            *(LAS u32x2*)(qb + 2 * lane) = u32x2{pk2(__uint_as_float(a[0].x) + __uint_as_float(c[0].x), __uint_as_float(a[0].z) + __uint_as_float(c[0].z)),
                                                 pk2(__uint_as_float(a[1].x) + __uint_as_float(c[1].x), __uint_as_float(a[1].z) + __uint_as_float(c[1].z))};
        }
        hbar();
        const u32x4 q0 = *(LAS u32x4*)(qb + 4 * c16), q1 = *(LAS u32x4*)(qb + 64 + 4 * c16);
        float x[16];
#pragma unroll
        for (int t = 0; t < 8; ++t) {
            float d0 = 0.f, d1 = 0.f;
#pragma unroll
            for (int k = 0; k < 4; ++k) { d0 = dot2(uk[t][k], q0[k], d0); d1 = dot2(uk[t][k], q1[k], d1); }
            x[2 * t] = d0; x[2 * t + 1] = d1;
        }
#define TR_STEP(HALF, CTRL, BIT) _Pragma("unroll") for (int hh = 0; hh < (HALF); ++hh) { \
            const float lo_ = x[hh], hi_ = x[hh + (HALF)]; const float t1_ = lo_ + dpp<CTRL>(lo_), t2_ = hi_ + dpp<CTRL>(hi_); x[hh] = (c16 & (BIT)) ? t2_ : t1_; }
        TR_STEP(8, XOR8, 8) TR_STEP(4, XOR7, 4) TR_STEP(2, XOR2, 2) TR_STEP(1, XOR1, 1)
#undef TR_STEP
        LAS u16* qa = (LAS u16*)(L + M_QA);
        qa[(c16 & 1) * 256 + 32 * w + 4 * (c16 >> 1) + r4] = (u16)(pk2(x[0], 0.f) & 0xffffu);
        hbar();
        if (w == 0) {
            st_sc1(rs, QABS_OFF + (((lane >> 5) * 32 + h) * 512 + 256 * j + 8 * (lane & 31)) * 2, *(LAS u32x4*)(qa + (lane >> 5) * 256 + 8 * (lane & 31)));
            VMCNT(0);
            st4_sc1(rs, QABS_FLAG + lane * 256 + (h * 2 + j) * 4, T);
        }
        hbar();
        if (w >= 4) fetch_q(L, rs, T, b, kvh, w, lane, t_start);
        hbar();
        bf16x8 uf[8];
        {
            const int q4 = (lane & 15) >> 2, p4 = lane & 3;
#pragma unroll
            for (int ks = 0; ks < 8; ++ks) {
                const unsigned row0 = 32 * ks + 8 * r4 + q4, row1 = row0 + 4, ch = 2 * w + (p4 >> 1);
                const s16x4 lo = trd(L + L_R0 + row0 * 256 + (ch ^ fswz(row0)) * 16 + 8 * (p4 & 1));
                const s16x4 hi = trd(L + L_R0 + row1 * 256 + (ch ^ fswz(row1)) * 16 + 8 * (p4 & 1));
                uf[ks] = __builtin_shufflevector(lo, hi, 0, 1, 2, 3, 4, 5, 6, 7);
            }
        }
        const int bb = w >> 2, s0 = 8 * (w & 3);
        if (w == 0) {
            for (;;) {
                const unsigned f = ld4_sc1(rs, BP_FLAG + (h * 2 + j) * 256 + lane * 4);
                if (__all(f == T)) break;
                __builtin_amdgcn_s_sleep(PSLEEP);
                if (spin_over(t_start)) break;
            }
        }
        hbar();
        const int hh = lane >> 5;
        const u32x2 st = ld8_sc1(rs, BST_OFF + ((bb * 32 + (lane & 31)) * 32 + h) * 8);
        u32x4 pd[4];
#pragma unroll
        for (int i = 0; i < 4; ++i) pd[i] = ld_sc1(rs, BP_OFF + ((bb * 32 + s0 + 2 * i + hh) * 32 + h) * 1024 + 512 * j + 16 * (lane & 31));
        const float ms = __uint_as_float(st.x), ls = __uint_as_float(st.y);
        float M = ms; M = fmaxf(M, dpp<XOR1>(M)); M = fmaxf(M, dpp<XOR2>(M)); M = fmaxf(M, dpp<ROR4>(M)); M = fmaxf(M, dpp<XOR8>(M)); M = x16_max(M);
        const float wt = __builtin_amdgcn_exp2f(ms - M);
        float Ls = wt * ls; Ls += dpp<XOR1>(Ls); Ls += dpp<XOR2>(Ls); Ls += dpp<ROR4>(Ls); Ls += dpp<XOR8>(Ls); Ls = x16_sum(Ls);
        const float wn = wt / Ls;
        float c8[8];
#pragma unroll
        for (int k = 0; k < 8; ++k) c8[k] = 0.f;
#pragma unroll
        for (int i = 0; i < 4; ++i) {
            const float wa = __builtin_bit_cast(float, __builtin_amdgcn_readlane(__builtin_bit_cast(int, wn), s0 + 2 * i));
            const float wb = __builtin_bit_cast(float, __builtin_amdgcn_readlane(__builtin_bit_cast(int, wn), s0 + 2 * i + 1));
            const float wi = hh ? wb : wa;
#pragma unroll
            for (int k = 0; k < 4; ++k) { c8[2 * k] = fmaf(wi, lo2f(pd[i][k]), c8[2 * k]); c8[2 * k + 1] = fmaf(wi, hi2f(pd[i][k]), c8[2 * k + 1]); }
        }
        f32x4 cc;
#pragma unroll
        for (int k = 0; k < 4; ++k) cc[k] = x32_red2(c8[k], c8[k + 4]);
        LAS float* ccr = (LAS float*)(L + M_CCR);
        *(LAS f32x4*)(ccr + w * 256 + 8 * (lane & 31) + 4 * hh) = cc;
        hbar();
        LAS u16* ccb = (LAS u16*)(L + M_RED);
        {
            const float v = (ccr[(tid >> 8) * 1024 + (tid & 255)] + ccr[(tid >> 8) * 1024 + 256 + (tid & 255)]) + (ccr[(tid >> 8) * 1024 + 512 + (tid & 255)] + ccr[(tid >> 8) * 1024 + 768 + (tid & 255)]);
            const float hi = rbf(v);
            const unsigned pk = pk2(hi, v - hi);
            ccb[tid] = (u16)(pk & 0xffffu); ccb[512 + tid] = (u16)(pk >> 16);
        }
        hbar();
        f32x4 d = {0.f, 0.f, 0.f, 0.f}, e = {0.f, 0.f, 0.f, 0.f};
#pragma unroll
        for (int ks = 0; ks < 8; ++ks) {
            const bf16x8 bh = *(LAS bf16x8*)(ccb + (lane & 1) * 256 + 32 * ks + 8 * r4);
            const bf16x8 bl = *(LAS bf16x8*)(ccb + 512 + (lane & 1) * 256 + 32 * ks + 8 * r4);
            d = mfma16(uf[ks], bh, d);
            e = mfma16(uf[ks], bl, e);
        }
        d += e;
        if (c16 < 2) {
            const int off = CTX_OFF + ((((h * 4 + (w >> 1)) * 2 + j) * 2 + c16) * 32 + 16 * (w & 1) + 4 * r4) * 8;
            st_sc1(rs, off, gran2(d.x, d.y, T));
            st_sc1(rs, off + 16, gran2(d.z, d.w, T));
        }
    } else {
        const int idx = pr * 2 + j, bb = idx >> 5;
        const int hf_w = w >> 2, wq4 = w & 3, hh = lane >> 5, l31 = lane & 31;
        if (w == 0) {
            for (;;) {
                const unsigned f = ld4_sc1(rs, QABS_FLAG + idx * 256 + lane * 4);
                if (__all(f == T)) break;
                __builtin_amdgcn_s_sleep(PSLEEP);
                if (spin_over(t_start)) break;
            }
        }
        hbar();
        u32x4 qv[4];
#pragma unroll
        for (int i = 0; i < 4; ++i) qv[i] = ld_sc1(rs, QABS_OFF + ((bb * 32 + 8 * wq4 + 2 * i + hh) * 512 + 256 * hf_w + 8 * l31) * 2);
        f32x16 sacc;
#pragma unroll
        for (int i = 0; i < 16; ++i) sacc[i] = 0.f;
#pragma unroll
        for (int hf = 0; hf < 2; ++hf) {
            if (hf_w == hf) {
#pragma unroll
                for (int i = 0; i < 4; ++i) *(LAS u32x4*)(L + L_QB + (8 * wq4 + 2 * i + hh) * 528 + l31 * 16) = qv[i];
            }
            hbar();
            if (w < 4) {
                const int row = 32 * w + l31;
                const unsigned f = fswz(row);
                bf16x8 A[2][4], B[2][4];
#pragma unroll
                for (int g = 0; g < 5; ++g) {
                    if (g < 4) {
#pragma unroll
                        for (int u = 0; u < 4; ++u) {
                            A[g & 1][u] = *(LAS bf16x8*)(L + L_R0 + row * 1024 + (((unsigned)(32 * hf + 2 * (4 * g + u) + hh)) ^ f) * 16);
                            B[g & 1][u] = *(LAS bf16x8*)(L + L_QB + l31 * 528 + (2 * (4 * g + u) + hh) * 16);
                        }
                    }
                    __builtin_amdgcn_sched_barrier(0);
                    if (g > 0) {
#pragma unroll
                        for (int u = 0; u < 4; ++u) sacc = __builtin_amdgcn_mfma_f32_32x32x16_bf16(A[(g - 1) & 1][u], B[(g - 1) & 1][u], sacc, 0, 0, 0);
                    }
                    __builtin_amdgcn_sched_barrier(0);
                }
            }
            if (hf == 0) hbar();
        }
        LAS float* rm = (LAS float*)(L + M_RED);
        LAS float* rl = (LAS float*)(L + M_RED + 512);
        if (w < 4) {
            float mx = sacc[0];
#pragma unroll
            for (int i = 1; i < 16; ++i) mx = fmaxf(mx, sacc[i]);
            mx = x32_max(mx);
            if (lane < 32) rm[w * 32 + lane] = mx;
        }
        hbar();
        const float M = fmaxf(fmaxf(rm[l31], rm[32 + l31]), fmaxf(rm[64 + l31], rm[96 + l31])) * C2;
        if (w < 4) {
            float ls = 0.f;
#pragma unroll
            for (int g = 0; g < 4; ++g) {
                float e[4];
#pragma unroll
                for (int i = 0; i < 4; ++i) { e[i] = __builtin_amdgcn_exp2f(fmaf(sacc[4 * g + i], C2, -M)); ls += e[i]; }
                *(LAS u32x2*)(L + L_QB + l31 * 272 + (32 * w + 8 * g + 4 * hh) * 2) = u32x2{pk2(e[0], e[1]), pk2(e[2], e[3])};
            }
            ls = x32_sum(ls);
            if (lane < 32) rl[w * 32 + lane] = ls;
        }
        hbar();
        bf16x8 pf[8];
#pragma unroll
        for (int ks = 0; ks < 8; ++ks) pf[ks] = *(LAS bf16x8*)(L + L_QB + l31 * 272 + (16 * ks + 8 * hh) * 2);
        const int blk = (lane >> 4) & 1, q4 = (lane & 15) >> 2, p4 = lane & 3;
        u32x2 ob[2][4];
        {
            f32x16 o[2];
#pragma unroll
            for (int i = 0; i < 16; ++i) { o[0][i] = 0.f; o[1][i] = 0.f; }
            bf16x8 A[2][4];
#pragma unroll
            for (int g = 0; g < 5; ++g) {
                if (g < 4) {
#pragma unroll
                    for (int u = 0; u < 4; ++u) {
                        const unsigned row0 = 16 * (2 * g + (u >> 1)) + 8 * hh + q4, row1 = row0 + 4;
                        const unsigned ch = 4 * (2 * w + (u & 1)) + 2 * blk + (p4 >> 1);
                        const s16x4 lo = trd(L + L_R0 + row0 * 1024 + (ch ^ fswz(row0)) * 16 + 8 * (p4 & 1));
                        const s16x4 hi = trd(L + L_R0 + row1 * 1024 + (ch ^ fswz(row1)) * 16 + 8 * (p4 & 1));
                        A[g & 1][u] = __builtin_shufflevector(lo, hi, 0, 1, 2, 3, 4, 5, 6, 7);
                    }
                }
                __builtin_amdgcn_sched_barrier(0);
                if (g > 0) {
#pragma unroll
                    for (int u = 0; u < 4; ++u) o[u & 1] = __builtin_amdgcn_mfma_f32_32x32x16_bf16(A[(g - 1) & 1][u], pf[2 * (g - 1) + (u >> 1)], o[u & 1], 0, 0, 0);
                }
                __builtin_amdgcn_sched_barrier(0);
            }
#pragma unroll
            for (int cti = 0; cti < 2; ++cti)
#pragma unroll
                for (int g = 0; g < 4; ++g) ob[cti][g] = u32x2{pk2(o[cti][4 * g], o[cti][4 * g + 1]), pk2(o[cti][4 * g + 2], o[cti][4 * g + 3])};
        }
        if (tid < 32) st8_sc1(rs, BST_OFF + (idx * 32 + tid) * 8, u32x2{__float_as_uint(M), __float_as_uint(rl[tid] + rl[32 + tid] + rl[64 + tid] + rl[96 + tid])});
        hbar();
#pragma unroll
        for (int cti = 0; cti < 2; ++cti)
#pragma unroll
            for (int g = 0; g < 4; ++g) *(LAS u32x2*)(L + L_R0 + l31 * 1040 + (32 * (2 * w + cti) + 8 * g + 4 * hh) * 2) = ob[cti][g];
        hbar();
#pragma unroll
        for (int i = 0; i < 4; ++i) st_sc1(rs, BP_OFF + (idx * 32 + 4 * w + i) * 1024 + lane * 16, *(LAS u32x4*)(L + L_R0 + (4 * w + i) * 1040 + lane * 16));
        VMCNT(0);
        hbar();
        if (w == 0) st4_sc1(rs, BP_FLAG + lane * 256 + idx * 4, T);
        if (w >= 4) fetch_q(L, rs, T, b, kvh, w, lane, t_start);
    }

    const int R = role == 2 ? pr * 2 + j : (pr < 4) ? 64 + pr * 2 + j : -1;
    if (R >= 0) {
        const int ob = R >= 36 ? 1 : 0;
        float gpart = 0.f;
        if (w == 0) {
#pragma unroll
            for (int i = 0; i < 9; ++i) gpart += rbf(bf2f(p.x[ob * DM + lane + 64 * i]) * bf2f(p.gw[lane + 64 * i]));
            gpart += dpp<XOR1>(gpart); gpart += dpp<XOR2>(gpart); gpart += dpp<ROR4>(gpart); gpart += dpp<XOR8>(gpart); gpart = x32_sum(x16_sum(gpart));
            gpart = rbf(1.f / (1.f + __expf(-rbf(gpart))));
        }
        lds_wait<8>(sync + 1, 16u, t_start);
        float v0, v1;
        for (;;) {
            const u32x4 gs = ld_sc1(rs, AST_OFF + ((2 * kvh * 2 + b) * 8 + (lane & 7)) * 16);
            if (__all(tagok(gs, T))) break;
            __builtin_amdgcn_s_sleep(4);
            if (spin_over(t_start)) break;
        }
        if (w < 4) {
            u32x4 g[4];
            fetch_all<4, 1024>(g, rs, OUTP_OFF + R * 32768 + w * 4096 + lane * 16, T, t_start);
            v0 = (__uint_as_float(g[0].x) + __uint_as_float(g[1].x)) + (__uint_as_float(g[2].x) + __uint_as_float(g[3].x));
            v1 = (__uint_as_float(g[0].z) + __uint_as_float(g[1].z)) + (__uint_as_float(g[2].z) + __uint_as_float(g[3].z));
        } else {
            u32x4 g[4];
            fetch_all<4, 1024>(g, rs, OUTP_OFF + R * 32768 + w * 4096 + lane * 16, T, t_start);
            v0 = (__uint_as_float(g[0].x) + __uint_as_float(g[1].x)) + (__uint_as_float(g[2].x) + __uint_as_float(g[3].x));
            v1 = (__uint_as_float(g[0].z) + __uint_as_float(g[1].z)) + (__uint_as_float(g[2].z) + __uint_as_float(g[3].z));
        }
        float v = x32_red2(v0, v1);
        v += dpp<XOR8>(v); v = x16_sum(v);
        LAS float* fin = (LAS float*)(L + M_FIN);
        if ((lane & 31) < 8) fin[w * 16 + 2 * (lane & 7) + (lane >> 5)] = v;
        gbar(gb);
        if (tid < 16) {
            const float sa = (fin[tid] + fin[16 + tid]) + (fin[32 + tid] + fin[48 + tid]);
            const float sb = (fin[64 + tid] + fin[80 + tid]) + (fin[96 + tid] + fin[112 + tid]);
            const float g = gpart;
            const float res = rbf(g * rbf(sa)) + rbf(rbf(1.f - g) * rbf(sb));
            p.out[R * 16 + tid] = (u16)(pk2(res, 0.f) & 0xffffu);
            if (R == 0 && tid == 0) st4_sc1(rs, EP_OFF, T);
        }
    }
}

extern "C" void kernel_launch(void* const* d_in, const int* in_sizes, int n_in, void* d_out, int out_size, void* d_ws, size_t ws_size, hipStream_t stream) {
    Params p;
    p.x = (const u16*)d_in[0]; p.kc = (const u16*)d_in[1]; p.vc = (const u16*)d_in[2]; p.wq = (const u16*)d_in[3]; p.wo = (const u16*)d_in[4];
    p.ckv = (const u16*)d_in[6]; p.w_q = (const u16*)d_in[7]; p.w_uk = (const u16*)d_in[8]; p.w_uv = (const u16*)d_in[9]; p.w_o = (const u16*)d_in[10];
    p.gw = (const u16*)d_in[11]; p.out = (u16*)d_out; p.ws = (unsigned char*)d_ws;
    (void)hipFuncSetAttribute((const void*)fused_decode, hipFuncAttributeMaxDynamicSharedMemorySize, LDS_BYTES);
    hipLaunchKernelGGL(fused_decode, dim3(256), dim3(1024), LDS_BYTES, stream, p);
}
```
